```python
import torch

def _softmax(x, axis=-1):
    return torch.softmax(x, dim=axis)
def _gelu(x):
    return torch.nn.functional.gelu(x, approximate="tanh")
def _silu(x):
    return torch.nn.functional.silu(x)
def _sigmoid(x):
    return torch.sigmoid(x)
def _layernorm(x, axis=-1, eps=1e-5):
    m = x.mean(dim=axis, keepdim=True)
    v = x.var(dim=axis, keepdim=True, unbiased=False)
    return (x - m) / torch.sqrt(v + eps)
def _scatter_add(tgt, idx, src, axis=0):
    return tgt.clone().index_add_(axis, idx, src)
def _segment_sum(src, seg, n):
    out = torch.zeros((n,) + src.shape[1:], dtype=src.dtype)
    return out.index_add_(0, seg, src)
def _pad(x, pw):
    flat = tuple(v for pair in reversed(pw) for v in pair)
    return torch.nn.functional.pad(x, flat)

kernel_name = 'sequential_preLN__mha_block__dense_activation_mish__prompt'

def setup_inputs():

    _w = (lambda a: (lambda t: t.to(torch.bfloat16) if t.is_floating_point() else t)(torch.as_tensor(a)))
    def _A_setup():
        B, S, D, H = 1, 2048, 512, 32
        return {
            "x": _w((lambda *s: torch.randn(*s, dtype=torch.bfloat16) * 0.05)(B, S, D)),
            "wqkv": _w((lambda *s: torch.randn(*s, dtype=torch.bfloat16) * 0.05)(D, 3 * D)),
            "wo": _w((lambda *s: torch.randn(*s, dtype=torch.bfloat16) * 0.05)(D, D)),
        }
    _A_in = _A_setup()

    def _B_setup():
        B, S, D, F = 1, 2048, 512, 1376
        return {
            "x": _w((lambda *s: torch.randn(*s, dtype=torch.bfloat16) * 0.05)(B, S, D)),
            "w1": _w((lambda *s: torch.randn(*s, dtype=torch.bfloat16) * 0.8)(D, F) * (D ** -0.5)),
            "b1": _w((lambda *s: torch.randn(*s, dtype=torch.bfloat16) * 0.8)(F)),
            "w2": _w((lambda *s: torch.randn(*s, dtype=torch.bfloat16) * 0.2)(F, D) * (F ** -0.5)),
        }
    _B_in = _B_setup()

    return {
        "A_x": _A_in["x"],
        "A_wqkv": _A_in["wqkv"],
        "A_wo": _A_in["wo"],
        "B_x": _B_in["x"],
        "B_w1": _B_in["w1"],
        "B_b1": _B_in["b1"],
        "B_w2": _B_in["w2"],
    }

def reference(A_x, A_wqkv, A_wo, B_x, B_w1, B_b1, B_w2):
    def _A_ref(x, wqkv, wo):
        B, S, D = x.shape
        H = 32
        Dh = D // H
        qkv = (lambda x, *d: x.permute(*d))((x @ wqkv).reshape(B, S, 3, H, Dh), 2, 0, 3, 1, 4)
        q, k, v = qkv[0], qkv[1], qkv[2]
        scores = torch.einsum("bhqd,bhkd->bhqk", q, k) * (Dh ** -0.5)
        p = _softmax(scores, axis=-1)
        o = torch.einsum("bhqk,bhkd->bhqd", p, v)
        return (lambda x, *d: x.permute(*d))(o, 0, 2, 1, 3).reshape(B, S, D) @ wo

    def _B_ref(x, w1, b1, w2):
        h = torch.einsum("bsd,df->bsf", x, w1) + b1
        sp = torch.log1p(torch.exp(-torch.abs(h))) + torch.where(h > 0, h, torch.zeros_like(h))
        act = h * torch.tanh(sp)
        return torch.einsum("bsf,fd->bsd", act, w2)

    x = A_x
    h = x + _A_ref(_layernorm(x, axis=-1), A_wqkv, A_wo)
    h2 = h + _B_ref(_layernorm(h, axis=-1), B_w1, B_b1, B_w2)
    return h2
```

```cpp
#include <hip/hip_runtime.h>
#include <hip/hip_bf16.h>

typedef __bf16 bf16x2 __attribute__((ext_vector_type(2)));
typedef __bf16 bf16x4 __attribute__((ext_vector_type(4)));
typedef __bf16 bf16x8 __attribute__((ext_vector_type(8)));
typedef __bf16 bf16x16 __attribute__((ext_vector_type(16)));
typedef float f32x2 __attribute__((ext_vector_type(2)));
typedef float f32x4 __attribute__((ext_vector_type(4)));
typedef float f32x8 __attribute__((ext_vector_type(8)));
typedef float f32x16 __attribute__((ext_vector_type(16)));
typedef unsigned u32x2 __attribute__((ext_vector_type(2)));
typedef unsigned u32x4 __attribute__((ext_vector_type(4)));
#define LAS __attribute__((address_space(3)))
#define GAS __attribute__((address_space(1)))
#define DEVI __device__ __forceinline__

constexpr int S_ = 2048, D_ = 512, H_ = 32, F_ = 1376;
constexpr size_t QF_OFF = 0, KV_OFF = 2u << 20, OF_OFF = 6u << 20, HF_OFF = 8u << 20, AF_OFF = 10u << 20;
constexpr float LN_EPS = 1e-5f;
constexpr float QSCALE = 0.25f * 1.4426950408889634f;

#define WAIT_V(n) asm volatile("s_waitcnt vmcnt(" #n ")" ::: "memory")
#define WAIT_L0() asm volatile("s_waitcnt lgkmcnt(0)" ::: "memory")
#define BAR() asm volatile("s_barrier" ::: "memory")

template <int N> DEVI void wait_v() { asm volatile("s_waitcnt vmcnt(%0)" ::"i"(N) : "memory"); }

DEVI unsigned lds_addr(const char* p) { return (unsigned)(size_t)(const LAS char*)p; }
DEVI void glds16(const void* g, char* l) {
  unsigned keep;
  const unsigned dst = __builtin_amdgcn_readfirstlane(lds_addr(l));
  asm volatile("s_mov_b32 %0, m0\n\ts_mov_b32 m0, %2\n\ts_nop 0\n\tglobal_load_lds_dwordx4 %1, off\n\ts_mov_b32 m0, %0" : "=&s"(keep) : "v"(g), "s"(dst) : "memory");
}
DEVI bf16x4 trread(const char* l) { return __builtin_amdgcn_ds_read_tr16_b64_v4bf16((LAS bf16x4*)l); }
DEVI bf16x8 cat8(bf16x4 a, bf16x4 b) { return __builtin_shufflevector(a, b, 0, 1, 2, 3, 4, 5, 6, 7); }
DEVI f32x4 mfma16(bf16x8 a, bf16x8 b, f32x4 c) { return __builtin_amdgcn_mfma_f32_16x16x32_bf16(a, b, c, 0, 0, 0); }
DEVI float dot2(bf16x2 a, bf16x2 b, float c) { return __builtin_amdgcn_fdot2_f32_bf16(a, b, c, false); }
DEVI bf16x2 pr(bf16x8 v, int i) { return bf16x2{v[2 * i], v[2 * i + 1]}; }
DEVI float sum8(bf16x8 v, float c) {
  const bf16x2 one = {(__bf16)1.0f, (__bf16)1.0f};
#pragma unroll
  for (int i = 0; i < 4; i++) c = dot2(pr(v, i), one, c);
  return c;
}
DEVI float sq8(bf16x8 v, float c) {
#pragma unroll
  for (int i = 0; i < 4; i++) c = dot2(pr(v, i), pr(v, i), c);
  return c;
}
DEVI float red_c(float v) {
  v += __shfl_xor(v, 16);
  v += __shfl_xor(v, 32);
  return v;
}
DEVI bf16x4 pack4(float a, float b, float c, float d) { return bf16x4{(__bf16)a, (__bf16)b, (__bf16)c, (__bf16)d}; }
DEVI void gload16(bf16x8& d, const void* p) { asm volatile("global_load_dwordx4 %0, %1, off" : "=v"(d) : "v"(p) : "memory"); }

__global__ void __launch_bounds__(512, 2) k_qkv(const __bf16* __restrict__ x, const __bf16* __restrict__ w, char* __restrict__ ws) {
  extern __shared__ __attribute__((aligned(16))) char smem[];
  const int b = blockIdx.x, xcd = b & 7, i5 = b >> 3;
  const int row0 = (xcd >> 1) * 512 + (i5 & 7) * 64, head0 = (xcd & 1) * 16 + (i5 >> 3) * 4;
  const int tid = threadIdx.x, l = tid & 63, wv = __builtin_amdgcn_readfirstlane(tid >> 6);
  const int wm = wv & 1, hq = wv >> 1;
  const int lr = l >> 3, lc = l & 7;
  const int xrow = 8 * wv + lr;
  const char* xsrc = (const char*)x + (size_t)(row0 + xrow) * 1024 + ((lc ^ ((xrow >> 1) & 7)) << 4);
  const int sw = ((xrow >> 1) & 1) | ((xrow >> 2) & 2);
  const char* wsrc = (const char*)w + (size_t)xrow * 3072 + head0 * 32 + (((((lc >> 1) ^ sw) << 1) | (lc & 1)) << 4);
  auto stage = [&](int c) {
    char* dst = smem + (c & 3) * 32768 + wv * 1024;
    glds16(xsrc + c * 128, dst);
#pragma unroll
    for (int t = 0; t < 3; t++) glds16(wsrc + (size_t)c * 64 * 3072 + t * 1024, dst + 8192 + t * 8192);
  };
  stage(0); stage(1); stage(2); stage(3);
  const int r = l & 15, c4 = l >> 4, q4 = (l & 15) >> 2, p4 = l & 3;
  const int sx = (r >> 1) & 7;
  const int xa0 = (32 * wm + r) * 128 + ((c4 ^ sx) << 4), xa1 = (32 * wm + r) * 128 + (((4 + c4) ^ sx) << 4);
  const int swr = ((q4 >> 1) & 1) | ((c4 & 1) << 1);
  const int wa = 8192 + (8 * c4 + q4) * 128 + ((hq ^ swr) << 5) + p4 * 8;
  f32x4 acc[3][2] = {};
  float rs[2] = {0.f, 0.f}, rq[2] = {0.f, 0.f}, cs[3] = {0.f, 0.f, 0.f};
  auto compute = [&](int c) {
    const char* base = smem + (c & 3) * 32768;
#pragma unroll
    for (int ks = 0; ks < 2; ks++) {
      bf16x8 xf[2], wf[3];
#pragma unroll
      for (int j = 0; j < 2; j++) xf[j] = *(const bf16x8*)(base + (ks ? xa1 : xa0) + j * 2048);
#pragma unroll
      for (int t = 0; t < 3; t++) wf[t] = cat8(trread(base + wa + t * 8192 + ks * 4096), trread(base + wa + t * 8192 + ks * 4096 + 512));
#pragma unroll
      for (int j = 0; j < 2; j++) {
        acc[0][j] = mfma16(wf[0], xf[j], acc[0][j]);
        acc[1][j] = mfma16(wf[1], xf[j], acc[1][j]);
        acc[2][j] = mfma16(xf[j], wf[2], acc[2][j]);
      }
      if (((2 * c + ks) & 3) == hq) {
#pragma unroll
        for (int j = 0; j < 2; j++) { rs[j] = sum8(xf[j], rs[j]); rq[j] = sq8(xf[j], rq[j]); }
      }
      if (ks == wm) {
#pragma unroll
        for (int t = 0; t < 3; t++) cs[t] = sum8(wf[t], cs[t]);
      }
    }
  };
#pragma unroll
  for (int c = 0; c < 8; c++) {
    if (c < 5) WAIT_V(12); else if (c == 5) WAIT_V(8); else if (c == 6) WAIT_V(4); else WAIT_V(0);
    BAR();
    compute(c);
    if (c < 4) { WAIT_L0(); BAR(); stage(c + 4); }
  }
  WAIT_L0(); BAR();
  float* rst = (float*)smem;
  float* cst = (float*)(smem + 4096);
#pragma unroll
  for (int j = 0; j < 2; j++) {
    float a = red_c(rs[j]), q = red_c(rq[j]);
    if (c4 == 0) *(f32x2*)(rst + (hq * 64 + 32 * wm + 16 * j + r) * 2) = f32x2{a, q};
  }
#pragma unroll
  for (int t = 0; t < 3; t++) {
    float a = red_c(cs[t]);
    if (c4 == 0) cst[((wm * 4 + hq) * 3 + t) * 16 + r] = a;
  }
  __syncthreads();
  float* fin = (float*)(smem + 8192);
  if (tid < 64) {
    float a = 0.f, q = 0.f;
#pragma unroll
    for (int k = 0; k < 4; k++) { f32x2 v = *(f32x2*)(rst + (k * 64 + tid) * 2); a += v.x; q += v.y; }
    float mean = a * (1.0f / D_);
    float var = fmaxf(q * (1.0f / D_) - mean * mean, 0.f);
    *(f32x2*)(fin + tid * 2) = f32x2{mean, __builtin_amdgcn_rsqf(var + LN_EPS)};
  }
  __syncthreads();
  const int head = head0 + hq, rb = row0 / 32 + wm;
#pragma unroll
  for (int t = 0; t < 2; t++) {
    f32x4 c0 = *(f32x4*)(cst + ((0 * 4 + hq) * 3 + t) * 16 + 4 * c4), c1 = *(f32x4*)(cst + ((1 * 4 + hq) * 3 + t) * 16 + 4 * c4);
    f32x4 cc = c0 + c1;
    char* dst = t == 0 ? ws + QF_OFF + (size_t)(head * 64 + rb) * 1024 : ws + KV_OFF + (size_t)(head * 64 + rb) * 2048;
#pragma unroll
    for (int j = 0; j < 2; j++) {
      f32x2 st = *(f32x2*)(fin + (32 * wm + 16 * j + r) * 2);
      float sc = t == 0 ? st.y * QSCALE : st.y;
      f32x4 v = (acc[t][j] - st.x * cc) * sc;
      *(bf16x4*)(dst + (c4 >> 1) * 512 + (16 * j + r) * 16 + (c4 & 1) * 8) = pack4(v.x, v.y, v.z, v.w);
    }
  }
  {
    float cc = cst[((0 * 4 + hq) * 3 + 2) * 16 + r] + cst[((1 * 4 + hq) * 3 + 2) * 16 + r];
    char* dst = ws + KV_OFF + (size_t)(head * 64 + rb) * 2048 + 1024;
#pragma unroll
    for (int j = 0; j < 2; j++) {
      f32x4 s0 = *(f32x4*)(fin + (32 * wm + 16 * j + 4 * c4) * 2), s1 = *(f32x4*)(fin + (32 * wm + 16 * j + 4 * c4 + 2) * 2);
      f32x4 a = acc[2][j];
      *(bf16x4*)(dst + j * 512 + (c4 >> 1) * 256 + r * 16 + (c4 & 1) * 8) =
          pack4((a.x - s0.x * cc) * s0.y, (a.y - s0.z * cc) * s0.w, (a.z - s1.x * cc) * s1.y, (a.w - s1.z * cc) * s1.w);
    }
  }
}

DEVI f32x16 mfma32(bf16x8 a, bf16x8 b, f32x16 c) { return __builtin_amdgcn_mfma_f32_32x32x16_bf16(a, b, c, 0, 0, 0); }
DEVI f32x16 pv(bf16x8 v0, bf16x8 v1, f32x16 p, f32x16 o) {
  f32x8 lo = __builtin_shufflevector(p, p, 0, 1, 2, 3, 4, 5, 6, 7), hi = __builtin_shufflevector(p, p, 8, 9, 10, 11, 12, 13, 14, 15);
  o = __builtin_amdgcn_smfmac_f32_32x32x32_bf16(v0, __builtin_bit_cast(bf16x16, lo), o, 0xDDDD, 0, 0);
  o = __builtin_amdgcn_smfmac_f32_32x32x32_bf16(v1, __builtin_bit_cast(bf16x16, hi), o, 0xDDDD, 0, 0);
  return o;
}
DEVI float max16(f32x16 s) {
  float m = fmaxf(fmaxf(s[0], s[1]), s[2]);
#pragma unroll
  for (int i = 3; i < 15; i += 2) m = fmaxf(fmaxf(m, s[i]), s[i + 1]);
  m = fmaxf(m, s[15]);
  return fmaxf(m, __shfl_xor(m, 32));
}

constexpr int KV_T = 2048 + 64;
__global__ void __launch_bounds__(512, 2) k_attn(char* __restrict__ ws) {
  extern __shared__ __attribute__((aligned(16))) char smem[];
  const int b = blockIdx.x, xcd = b & 7, i5 = b >> 3;
  const int head = (xcd & 1) * 16 + (xcd >> 1) * 4 + (i5 & 3), qb = i5 >> 2;
  const int tid = threadIdx.x, l = tid & 63, wv = __builtin_amdgcn_readfirstlane(tid >> 6);
  const int rb = qb * 8 + wv;
  bf16x8 q;
  gload16(q, ws + QF_OFF + (size_t)(head * 64 + rb) * 1024 + l * 16);
  const char* src = ws + KV_OFF + (size_t)head * 131072 + wv * 1024 + l * 16;
#pragma unroll
  for (int p = 0; p < 16; p++) glds16(src + p * 8192, smem + (p * 4 + (wv >> 1)) * KV_T + (wv & 1) * 1024);
  if (tid < 256) {
    unsigned one = (tid & 2) ? 0u : 0x3F803F80u;
    *(u32x4*)(smem + (tid >> 2) * KV_T + 2048 + (tid & 3) * 16) = u32x4{one, one, one, one};
  }
  const int r = l & 31, hh = l >> 5;
  const int ka = l * 16;
  const bool real = r < 16;
  const int cofs = ((r & 11) == 0) ? 2048 : 2080;
  const int va0 = real ? 1024 + hh * 256 + r * 16 : cofs, va1 = real ? 1536 + hh * 256 + r * 16 : cofs;
  asm volatile("s_waitcnt vmcnt(15) lgkmcnt(0)" : "+v"(q)::"memory");
  BAR();
  f32x16 zero = {};
  f32x16 s = mfma32(*(const bf16x8*)(smem + ka), q, zero);
  const float m0 = max16(s);
  f32x16 cm;
#pragma unroll
  for (int i = 0; i < 16; i++) cm[i] = -m0;
  asm volatile("" : "+v"(cm));
  s = s + cm;
  f32x16 o = {};
  auto tile = [&](int t, bool last) {
    const char* base = smem + t * KV_T;
    bf16x8 v0 = *(const bf16x8*)(base + va0), v1 = *(const bf16x8*)(base + va1);
    f32x16 sn;
    if (!last) sn = mfma32(*(const bf16x8*)(base + KV_T + ka), q, cm);
    f32x16 p;
#pragma unroll
    for (int i = 0; i < 16; i++) p[i] = __builtin_amdgcn_exp2f(s[i]);
    o = pv(v0, v1, p, o);
    if (!last) s = sn;
  };
  for (int t = 0; t < 3; t++) tile(t, false);
  WAIT_V(12); BAR();
#pragma unroll 2
  for (int t = 3; t < 15; t++) tile(t, false);
  WAIT_V(0); BAR();
#pragma unroll 4
  for (int t = 15; t < 63; t++) tile(t, false);
  tile(63, true);
  float lsum = o[8];
  float amax = fabsf(o[0]);
#pragma unroll
  for (int i = 1; i < 8; i++) amax = fmaxf(amax, fabsf(o[i]));
  const bool ok = (lsum < 1e18f) && (amax < 3e38f) && (o[0] + o[1] + o[2] + o[3] + o[4] + o[5] + o[6] + o[7] == o[0] + o[1] + o[2] + o[3] + o[4] + o[5] + o[6] + o[7]);
  if (!__all(ok)) {
    float m = -INFINITY;
    o = zero;
    for (int t = 0; t < 64; t++) {
      const char* base = smem + t * KV_T;
      f32x16 sv = mfma32(*(const bf16x8*)(base + ka), q, zero);
      float mn = fmaxf(m, max16(sv));
      float al = __builtin_amdgcn_exp2f(m - mn);
      m = mn;
      f32x16 p;
#pragma unroll
      for (int i = 0; i < 16; i++) p[i] = __builtin_amdgcn_exp2f(sv[i] - mn);
      o = o * al;
      o = pv(*(const bf16x8*)(base + va0), *(const bf16x8*)(base + va1), p, o);
    }
    lsum = o[8];
  }
  const float inv = 1.0f / lsum;
  bf16x8 ov;
#pragma unroll
  for (int i = 0; i < 8; i++) ov[i] = (__bf16)(o[i] * inv);
  *(bf16x8*)(ws + OF_OFF + (size_t)(rb * 32 + head) * 1024 + l * 16) = ov;
}

template <int MODE>
__global__ void __launch_bounds__(512, 2) k_proj(const __bf16* __restrict__ x, const __bf16* __restrict__ w, char* __restrict__ ws, __bf16* __restrict__ out) {
  extern __shared__ __attribute__((aligned(16))) char smem[];
  constexpr int NK = MODE ? 43 : 16;
  constexpr int KH = (NK + 1) / 2;
  constexpr int NST = (KH + 1) / 2;
  constexpr int NSL = NST < 9 ? NST : 9;
  const int b = blockIdx.x, xcd = b & 7, i5 = b >> 3;
  const int row0 = xcd * 256 + (i5 & 3) * 64, nt = i5 >> 2;
  const int tid = threadIdx.x, l = tid & 63, wv = __builtin_amdgcn_readfirstlane(tid >> 6);
  const int tt = wv & 3, kh = wv >> 2;
  const int r = l & 15, c4 = l >> 4, q4 = (l & 15) >> 2, p4 = l & 3;
  const int tile16 = row0 / 16 + tt;
  const int lr = l >> 3, lc = l & 7;
  const int srow = 8 * wv + lr;
  const char* wsrc = (const char*)w + nt * 128 + (((((lc >> 1) ^ ((lr >> 1) & 3)) << 1) | (lc & 1)) << 4);
  auto stage = [&](int st) {
#pragma unroll
    for (int h2 = 0; h2 < 2; h2++) {
      int row = h2 * KH * 32 + st * 64 + srow;
      row = row < NK * 32 ? row : NK * 32 - 1;
      glds16(wsrc + (size_t)row * 1024, smem + (st % NSL) * 16384 + h2 * 8192 + wv * 1024);
    }
  };
  const char* asrc = MODE ? ws + AF_OFF + (size_t)tile16 * 43 * 1024 + l * 16
                          : ws + OF_OFF + (size_t)(tile16 >> 1) * 32768 + (c4 >> 1) * 1024 + (c4 & 1) * 512 + (16 * (tile16 & 1) + r) * 16;
  constexpr int ASTR = MODE ? 1024 : 2048;
  bf16x8 af[2 * NST];
  auto aload = [&](int st) {
#pragma unroll
    for (int u = 0; u < 2; u++) {
      int ks = kh * KH + 2 * st + u;
      ks = ks < NK ? ks : NK - 1;
      gload16(af[2 * st + u], asrc + (size_t)ks * ASTR);
    }
  };
#pragma unroll
  for (int st = 0; st < NSL; st++) { aload(st); stage(st); }
#pragma unroll
  for (int st = NSL; st < NST; st++) aload(st);
  const int pair = nt * 2 + kh;
  bf16x8 res;
  bf16x4 rx0, rx1;
  if (MODE) gload16(res, ws + HF_OFF + (size_t)(tile16 * 16 + pair) * 1024 + l * 16);
  else {
    const char* xp = (const char*)x + (size_t)(tile16 * 16 + r) * 1024 + pair * 64 + c4 * 8;
    asm volatile("global_load_dwordx2 %0, %2, off\n\tglobal_load_dwordx2 %1, %2, off offset:32" : "=&v"(rx0), "=&v"(rx1) : "v"(xp) : "memory");
  }
  constexpr int NRES = MODE ? 1 : 2;
  const int sw = ((c4 & 1) << 1) | (q4 >> 1);
  const int rowl = MODE ? 4 * c4 + q4 : 16 * (c4 >> 1) + 4 * (c4 & 1) + q4;
  constexpr int TOFS = MODE ? 16 * 128 : 8 * 128;
  int wa[4];
#pragma unroll
  for (int j = 0; j < 4; j++) wa[j] = kh * 8192 + rowl * 128 + ((j ^ sw) << 5) + p4 * 8;
  f32x4 acc[4] = {};
  auto compute = [&](int st) {
    const char* base = smem + (st % NSL) * 16384;
#pragma unroll
    for (int u = 0; u < 2; u++) {
      const bool valid0 = 2 * st + u < KH, valid1 = KH + 2 * st + u < NK;
      if (valid0 && (valid1 || kh == 0)) {
#pragma unroll
        for (int j = 0; j < 4; j++) {
          bf16x8 wf = cat8(trread(base + wa[j] + u * 4096), trread(base + wa[j] + u * 4096 + TOFS));
          acc[j] = mfma16(wf, af[2 * st + u], acc[j]);
        }
      }
    }
  };
  constexpr int TOT0 = 4 * NSL + 2 * (NST - NSL) + NRES;
#pragma unroll
  for (int st = 0; st < NST; st++) {
    constexpr int NREF = NST - NSL;
    const int issued = TOT0 + 2 * (st < NREF ? st : NREF);
    const int need = st < NSL ? 4 * (st + 1) : TOT0 + 2 * (st - NSL + 1);
    const int n = issued - need;
    asm volatile("s_waitcnt vmcnt(%2)" : "+v"(af[2 * st]), "+v"(af[2 * st + 1]) : "i"(n) : "memory");
    BAR();
    compute(st);
    if (st < NREF) { WAIT_L0(); BAR(); stage(st + NSL); }
  }
  if (MODE) asm volatile("s_waitcnt vmcnt(0) lgkmcnt(0)" : "+v"(res)::"memory");
  else asm volatile("s_waitcnt vmcnt(0) lgkmcnt(0)" : "+v"(rx0), "+v"(rx1)::"memory");
  BAR();
  f32x4* ex = (f32x4*)smem;
  ex[(wv * 2 + 0) * 64 + l] = kh ? acc[0] : acc[2];
  ex[(wv * 2 + 1) * 64 + l] = kh ? acc[1] : acc[3];
  __syncthreads();
  f32x4 v0 = (kh ? acc[2] : acc[0]) + ex[((wv ^ 4) * 2 + 0) * 64 + l];
  f32x4 v1 = (kh ? acc[3] : acc[1]) + ex[((wv ^ 4) * 2 + 1) * 64 + l];
  if (MODE) {
    bf16x4 o0 = pack4(v0.x + (float)res[0], v0.y + (float)res[1], v0.z + (float)res[2], v0.w + (float)res[3]);
    bf16x4 o1 = pack4(v1.x + (float)res[4], v1.y + (float)res[5], v1.z + (float)res[6], v1.w + (float)res[7]);
    char* op = (char*)out + (size_t)(tile16 * 16 + r) * 1024 + pair * 64 + c4 * 8;
    *(bf16x4*)op = o0;
    *(bf16x4*)(op + 32) = o1;
  } else {
    bf16x4 o0 = pack4(v0.x + (float)rx0[0], v0.y + (float)rx0[1], v0.z + (float)rx0[2], v0.w + (float)rx0[3]);
    bf16x4 o1 = pack4(v1.x + (float)rx1[0], v1.y + (float)rx1[1], v1.z + (float)rx1[2], v1.w + (float)rx1[3]);
    *(bf16x8*)(ws + HF_OFF + (size_t)(tile16 * 16 + pair) * 1024 + l * 16) = cat8(o0, o1);
  }
}

DEVI float mish(float v) {
  float e = __builtin_amdgcn_exp2f(fminf(v, 30.f) * 1.4426950408889634f);
  float n = e * (e + 2.0f);
  return v * n * __builtin_amdgcn_rcpf(n + 2.0f);
}
__global__ void __launch_bounds__(512, 2) k_up(const __bf16* __restrict__ w1, const __bf16* __restrict__ b1, char* __restrict__ ws) {
  extern __shared__ __attribute__((aligned(16))) char smem[];
  const int b = blockIdx.x, xcd = b & 7, i5 = b >> 3;
  const int row0 = xcd * 256 + (i5 & 1) * 128, ct = i5 >> 1;
  const int tid = threadIdx.x, l = tid & 63, wv = __builtin_amdgcn_readfirstlane(tid >> 6);
  const int r = l & 15, c4 = l >> 4, q4 = (l & 15) >> 2, p4 = l & 3;
  const int tile16 = row0 / 16 + wv;
  const int nun = ct < 14 ? 3 : 1;
  const char* wsrc[3];
#pragma unroll
  for (int u = 0; u < 3; u++) {
    const int g = u * 64 + l, rr = g / 12, ch = g % 12;
    int sch = (((ch >> 1) ^ ((rr >> 2) & 1)) << 1) | (ch & 1);
    sch = sch < nun * 4 ? sch : (sch & 3);
    wsrc[u] = (const char*)w1 + (size_t)(wv * 16 + rr) * (F_ * 2) + ct * 192 + sch * 16;
  }
  const char* asrc = ws + HF_OFF + (size_t)tile16 * 16384 + l * 16;
  bf16x8 af[16];
#pragma unroll
  for (int st = 0; st < 4; st++) {
#pragma unroll
    for (int u = 0; u < 4; u++) gload16(af[4 * st + u], asrc + (4 * st + u) * 1024);
#pragma unroll
    for (int u = 0; u < 3; u++) glds16(wsrc[u] + (size_t)st * 128 * (F_ * 2), smem + st * 24576 + wv * 3072 + u * 1024);
  }
  bf16x4 bias[6];
#pragma unroll
  for (int j = 0; j < 6; j++) {
    const int jj = j < 2 * nun ? j : (j & 1);
    const char* bp = (const char*)b1 + (ct * 96 + jj * 16 + 4 * c4) * 2;
    asm volatile("global_load_dwordx2 %0, %1, off" : "=v"(bias[j]) : "v"(bp) : "memory");
  }
  const int rowl = 4 * c4 + q4;
  const int we = rowl * 192 + (c4 & 1) * 32 + p4 * 8, wo = rowl * 192 - (c4 & 1) * 32 + p4 * 8;
  f32x4 acc[6] = {};
  float rs = 0.f, rq = 0.f, cs[6] = {};
#pragma unroll
  for (int st = 0; st < 4; st++) {
    asm volatile("s_waitcnt vmcnt(%4)" : "+v"(af[4 * st]), "+v"(af[4 * st + 1]), "+v"(af[4 * st + 2]), "+v"(af[4 * st + 3]) : "i"(7 * (3 - st) + 6) : "memory");
    BAR();
#pragma unroll
    for (int u = 0; u < 4; u++) {
      const int ks = 4 * st + u;
      const char* base = smem + ks * 6144;
      bf16x8 wf[6];
#pragma unroll
      for (int j = 0; j < 6; j++) {
        const char* a = base + ((j & 1) ? wo : we) + j * 32;
        wf[j] = cat8(trread(a), trread(a + 16 * 192));
        acc[j] = mfma16(wf[j], af[ks], acc[j]);
      }
      rs = sum8(af[ks], rs);
      rq = sq8(af[ks], rq);
      if ((ks & 7) == wv) {
#pragma unroll
        for (int j = 0; j < 6; j++) cs[j] = sum8(wf[j], cs[j]);
      }
    }
  }
  asm volatile("s_waitcnt vmcnt(0) lgkmcnt(0)" : "+v"(bias[0]), "+v"(bias[1]), "+v"(bias[2]), "+v"(bias[3]), "+v"(bias[4]), "+v"(bias[5])::"memory");
  float* cst = (float*)(smem + 98304);
#pragma unroll
  for (int j = 0; j < 6; j++) {
    float a = red_c(cs[j]);
    if (c4 == 0) cst[wv * 96 + j * 16 + r] = a;
  }
  rs = red_c(rs); rq = red_c(rq);
  const float mean = rs * (1.0f / D_);
  const float rstd = __builtin_amdgcn_rsqf(fmaxf(rq * (1.0f / D_) - mean * mean, 0.f) + LN_EPS);
  __syncthreads();
#pragma unroll
  for (int u = 0; u < 3; u++) {
    if (u < nun) {
      bf16x4 o[2];
#pragma unroll
      for (int e = 0; e < 2; e++) {
        const int j = 2 * u + e;
        f32x4 cc = {};
#pragma unroll
        for (int k = 0; k < 8; k++) cc += *(f32x4*)(cst + k * 96 + j * 16 + 4 * c4);
        f32x4 v = (acc[j] - mean * cc) * rstd;
        o[e] = pack4(mish(v.x + (float)bias[j][0]), mish(v.y + (float)bias[j][1]), mish(v.z + (float)bias[j][2]), mish(v.w + (float)bias[j][3]));
      }
      *(bf16x8*)(ws + AF_OFF + (size_t)(tile16 * 43 + ct * 3 + u) * 1024 + l * 16) = cat8(o[0], o[1]);
    }
  }
}

extern "C" void kernel_launch(void* const* d_in, const int* in_sizes, int n_in, void* d_out, int out_size, void* d_ws, size_t ws_size, hipStream_t stream) {
  const __bf16* x = (const __bf16*)d_in[0];
  const __bf16* wqkv = (const __bf16*)d_in[1];
  const __bf16* wo = (const __bf16*)d_in[2];
  const __bf16* w1 = (const __bf16*)d_in[4];
  const __bf16* b1 = (const __bf16*)d_in[5];
  const __bf16* w2 = (const __bf16*)d_in[6];
  char* ws = (char*)d_ws;
  constexpr int L1 = 131072, L2 = 64 * KV_T, L3 = 65536, L4 = 98304 + 3072, L5 = 9 * 16384;
  (void)hipFuncSetAttribute((const void*)k_qkv, hipFuncAttributeMaxDynamicSharedMemorySize, L1);
  (void)hipFuncSetAttribute((const void*)k_attn, hipFuncAttributeMaxDynamicSharedMemorySize, L2);
  (void)hipFuncSetAttribute((const void*)k_proj<0>, hipFuncAttributeMaxDynamicSharedMemorySize, L3);
  (void)hipFuncSetAttribute((const void*)k_up, hipFuncAttributeMaxDynamicSharedMemorySize, L4);
  (void)hipFuncSetAttribute((const void*)k_proj<1>, hipFuncAttributeMaxDynamicSharedMemorySize, L5);
  k_qkv<<<256, 512, L1, stream>>>(x, wqkv, ws);
  k_attn<<<256, 512, L2, stream>>>(ws);
  k_proj<0><<<256, 512, L3, stream>>>(x, wo, ws, (__bf16*)d_out);
  k_up<<<240, 512, L4, stream>>>(w1, b1, ws);
  k_proj<1><<<256, 512, L5, stream>>>(x, w2, ws, (__bf16*)d_out);
}
```

```cpp
#include <hip/hip_runtime.h>
#include <hip/hip_bf16.h>

typedef __bf16 bf16x2 __attribute__((ext_vector_type(2)));
typedef __bf16 bf16x4 __attribute__((ext_vector_type(4)));
typedef __bf16 bf16x8 __attribute__((ext_vector_type(8)));
typedef __bf16 bf16x16 __attribute__((ext_vector_type(16)));
typedef float f32x2 __attribute__((ext_vector_type(2)));
typedef float f32x4 __attribute__((ext_vector_type(4)));
typedef float f32x8 __attribute__((ext_vector_type(8)));
typedef float f32x16 __attribute__((ext_vector_type(16)));
typedef unsigned u32x2 __attribute__((ext_vector_type(2)));
typedef unsigned u32x4 __attribute__((ext_vector_type(4)));
#define LAS __attribute__((address_space(3)))
#define GAS __attribute__((address_space(1)))
#define DEVI __device__ __forceinline__

constexpr int S_ = 2048, D_ = 512, H_ = 32, F_ = 1376;
constexpr size_t QF_OFF = 0, KV_OFF = 2u << 20, OF_OFF = 6u << 20, HF_OFF = 8u << 20, AF_OFF = 10u << 20, CNT_OFF = 16u << 20;
constexpr float LN_EPS = 1e-5f;
constexpr float LOG2E = 1.4426950408889634f;
constexpr float QSCALE = 0.25f * LOG2E;
constexpr int LDS_BYTES = 163840;

#define STAMP(k, s)
#define STAMP_DECL
#define STAMP_FLUSH(k)

#define SB0() __builtin_amdgcn_sched_barrier(0)
template <int N> DEVI void wait_v() { asm volatile("s_waitcnt vmcnt(%0)" ::"i"(N) : "memory"); }
template <int N> DEVI void wait_v_bar() { asm volatile("s_waitcnt vmcnt(%0) lgkmcnt(0)\n\ts_barrier" ::"i"(N) : "memory"); }
DEVI void bar_l() { asm volatile("s_waitcnt lgkmcnt(0)\n\ts_barrier" ::: "memory"); }

DEVI void glds16(const void* base, unsigned voff, unsigned dst) {
  unsigned keep;
  asm volatile("s_mov_b32 %0, m0\n\ts_mov_b32 m0, %3\n\ts_nop 0\n\tglobal_load_lds_dwordx4 %1, %2\n\ts_mov_b32 m0, %0"
               : "=&s"(keep) : "v"(voff), "s"(base), "s"(dst) : "memory");
}
DEVI void gld16(bf16x8& d, const void* base, unsigned voff) { asm volatile("global_load_dwordx4 %0, %1, %2" : "=v"(d) : "v"(voff), "s"(base) : "memory"); }
DEVI void gld16_sc1(bf16x8& d, const void* base, unsigned voff) { asm volatile("global_load_dwordx4 %0, %1, %2 sc1" : "=v"(d) : "v"(voff), "s"(base) : "memory"); }
DEVI void gld8(bf16x4& d, const void* base, unsigned voff) { asm volatile("global_load_dwordx2 %0, %1, %2" : "=v"(d) : "v"(voff), "s"(base) : "memory"); }
DEVI void gst16_sc1(bf16x8 d, void* base, unsigned voff) { asm volatile("global_store_dwordx4 %0, %1, %2 sc1\n\ts_nop 1" ::"v"(voff), "v"(d), "s"(base) : "memory"); }

DEVI bf16x4 trread(unsigned a) { return __builtin_amdgcn_ds_read_tr16_b64_v4bf16((LAS bf16x4*)(size_t)a); }
DEVI bf16x8 ldsr16(unsigned a) { return *(const LAS bf16x8*)(size_t)a; }
template <class T> DEVI T ldsr(unsigned a) { return *(const LAS T*)(size_t)a; }
template <class T> DEVI void ldsw(unsigned a, T v) { *(LAS T*)(size_t)a = v; }
DEVI bf16x8 cat8(bf16x4 a, bf16x4 b) { return __builtin_shufflevector(a, b, 0, 1, 2, 3, 4, 5, 6, 7); }
DEVI f32x4 mfma16(bf16x8 a, bf16x8 b, f32x4 c) { return __builtin_amdgcn_mfma_f32_16x16x32_bf16(a, b, c, 0, 0, 0); }
DEVI void MF(f32x4& c, const bf16x8& a, const bf16x8& b) { asm volatile("v_mfma_f32_16x16x32_bf16 %0, %1, %2, %0" : "+v"(c) : "v"(a), "v"(b)); }
#define MF_PAD() asm volatile("s_nop 7" ::: "memory")
DEVI f32x16 mfma32(bf16x8 a, bf16x8 b, f32x16 c) { return __builtin_amdgcn_mfma_f32_32x32x16_bf16(a, b, c, 0, 0, 0); }
DEVI float dot2(bf16x2 a, bf16x2 b, float c) { return __builtin_amdgcn_fdot2_f32_bf16(a, b, c, false); }
DEVI bf16x2 pr(bf16x8 v, int i) { return bf16x2{v[2 * i], v[2 * i + 1]}; }
DEVI float sum8(bf16x8 v, float c) {
  const bf16x2 one = {(__bf16)1.0f, (__bf16)1.0f};
#pragma unroll
  for (int i = 0; i < 4; i++) c = dot2(pr(v, i), one, c);
  return c;
}
DEVI float sq8(bf16x8 v, float c) {
#pragma unroll
  for (int i = 0; i < 4; i++) c = dot2(pr(v, i), pr(v, i), c);
  return c;
}
DEVI float add_x32(float v) { auto r = __builtin_amdgcn_permlane32_swap(__float_as_uint(v), __float_as_uint(v), false, false); return __uint_as_float(r[0]) + __uint_as_float(r[1]); }
DEVI float add_x16(float v) { auto r = __builtin_amdgcn_permlane16_swap(__float_as_uint(v), __float_as_uint(v), false, false); return __uint_as_float(r[0]) + __uint_as_float(r[1]); }
DEVI float max_x32(float v) { auto r = __builtin_amdgcn_permlane32_swap(__float_as_uint(v), __float_as_uint(v), false, false); return fmaxf(__uint_as_float(r[0]), __uint_as_float(r[1])); }
DEVI float red_c(float v) { return add_x32(add_x16(v)); }
template <int CTRL> DEVI float dpp_add(float v) { return v + __int_as_float(__builtin_amdgcn_update_dpp(0, __float_as_int(v), CTRL, 0xF, 0xF, true)); }
DEVI float red8(float v) { return dpp_add<0x141>(dpp_add<0x4E>(dpp_add<0xB1>(v))); }
DEVI bf16x4 pack4(f32x4 v) { return bf16x4{(__bf16)v.x, (__bf16)v.y, (__bf16)v.z, (__bf16)v.w}; }
DEVI f32x4 up4(bf16x4 v) { return f32x4{(float)v[0], (float)v[1], (float)v[2], (float)v[3]}; }

DEVI bf16x8 swap_pair(bf16x4 a, bf16x4 b) {
  const u32x2 x = __builtin_bit_cast(u32x2, a), y = __builtin_bit_cast(u32x2, b);
  const auto s0 = __builtin_amdgcn_permlane16_swap(x[0], y[0], false, false), s1 = __builtin_amdgcn_permlane16_swap(x[1], y[1], false, false);
  return __builtin_bit_cast(bf16x8, u32x4{s0[0], s1[0], s0[1], s1[1]});
}

__global__ void __launch_bounds__(512, 2) k_qkv(const __bf16* __restrict__ x, const __bf16* __restrict__ w, char* __restrict__ ws) {
  STAMP(0, 0);
  const int b = blockIdx.x, xcd = b & 7, i5 = b >> 3;
  const int row0 = (xcd >> 1) * 512 + (i5 & 7) * 64, head0 = (xcd & 1) * 16 + (i5 >> 3) * 4;
  const int tid = threadIdx.x, l = tid & 63, wv = __builtin_amdgcn_readfirstlane(tid >> 6);
  if (wv >= 4) {
    const int lr = l >> 3, lc = l & 7, lw = wv & 3;
    unsigned xoff[2], woff[2];
#pragma unroll
    for (int u = 0; u < 2; u++) {
      const int xrow = 16 * lw + 8 * u + lr;
      xoff[u] = xrow * 1024 + ((lc ^ ((xrow >> 1) & 7)) << 4);
      woff[u] = xrow * 3072 + (((((lc >> 1) ^ (((xrow >> 1) & 1) | ((xrow >> 2) & 2))) << 1) | (lc & 1)) << 4);
    }
    const char* xb = (const char*)x + (size_t)row0 * 1024;
    const char* wb = (const char*)w + head0 * 32;
    auto stage = [&](int c, int slot) {
      const unsigned dst = slot * 32768 + lw * 2048;
#pragma unroll
      for (int u = 0; u < 2; u++) {
        glds16(xb + c * 128, xoff[u], dst + u * 1024);
#pragma unroll
        for (int t = 0; t < 3; t++) glds16(wb + c * 64 * 3072 + t * 1024, woff[u], dst + u * 1024 + 8192 + t * 8192);
      }
    };
    stage(0, 0); stage(1, 1); stage(2, 2);
#pragma unroll 1
    for (int c = 0; c < 5; c++) {
      wait_v_bar<16>();
      stage(c + 3, c < 2 ? c + 3 : c - 2);
    }
    wait_v_bar<16>();
    wait_v_bar<8>();
    wait_v_bar<0>();
    if (b == 0 && wv == 4) __hip_atomic_store((GAS unsigned*)(ws + CNT_OFF) + l * 64, 0u, __ATOMIC_RELAXED, __HIP_MEMORY_SCOPE_AGENT);
    return;
  }
  const int hq = wv;
  const int r = l & 15, c4 = l >> 4, q4 = (l & 15) >> 2, p4 = l & 3;
  const int sx = (r >> 1) & 7, swr = ((q4 >> 1) & 1) | ((c4 & 1) << 1);
  unsigned xa[2];
#pragma unroll
  for (int u = 0; u < 2; u++) xa[u] = r * 128 + (((4 * u + c4) ^ sx) << 4);
  const unsigned wa = 8192 + (8 * c4 + q4) * 128 + ((hq ^ swr) << 5) + p4 * 8;
  f32x4 acc[3][4] = {}, cac[3] = {}, sac = {}, gac = {};
  bf16x8 ones = __builtin_bit_cast(bf16x8, u32x4{0x3F803F80u, 0x3F803F80u, 0x3F803F80u, 0x3F803F80u});
  asm volatile("" : "+v"(ones));
  bf16x8 xf[2][4], wf[2][3];
  unsigned xj[2][4];
#pragma unroll
  for (int u = 0; u < 2; u++)
#pragma unroll
    for (int j = 0; j < 4; j++) xj[u][j] = xa[u] + (j ^ hq) * 2048;
  auto rd = [&](int u, int p, unsigned base) {
    if (p == 0) xf[u][0] = ldsr16(base + xj[u][0]);
    else if (p < 4) wf[u][p - 1] = cat8(trread(base + wa + (p - 1) * 8192 + u * 4096), trread(base + wa + (p - 1) * 8192 + u * 4096 + 512));
    else xf[u][p - 3] = ldsr16(base + xj[u][p - 3]);
    SB0();
  };
  auto mm = [&](int u, bool pre, unsigned base) {
    const int v = u ^ 1;
    MF(sac, ones, xf[u][0]); if (pre) rd(v, 0, base);
    MF(gac, xf[u][0], xf[u][0]);
    MF(cac[0], wf[u][0], ones); if (pre) rd(v, 1, base);
    MF(acc[0][0], wf[u][0], xf[u][0]);
    MF(cac[1], wf[u][1], ones); if (pre) rd(v, 2, base);
    MF(acc[1][0], wf[u][1], xf[u][0]);
    MF(cac[2], ones, wf[u][2]); if (pre) rd(v, 3, base);
    MF(acc[2][0], xf[u][0], wf[u][2]);
#pragma unroll
    for (int j = 1; j < 4; j++) {
      MF(acc[0][j], wf[u][0], xf[u][j]); if (pre && j == 1) rd(v, 4, base);
      MF(acc[1][j], wf[u][1], xf[u][j]); if (pre && j == 1) rd(v, 5, base);
      MF(acc[2][j], xf[u][j], wf[u][2]); if (pre && j == 1) rd(v, 6, base);
    }
    SB0();
  };
  asm volatile("" : "+v"(acc[0][0]), "+v"(acc[0][1]), "+v"(acc[0][2]), "+v"(acc[0][3]), "+v"(acc[1][0]), "+v"(acc[1][1]), "+v"(acc[1][2]), "+v"(acc[1][3]),
               "+v"(acc[2][0]), "+v"(acc[2][1]), "+v"(acc[2][2]), "+v"(acc[2][3]), "+v"(cac[0]), "+v"(cac[1]), "+v"(cac[2]), "+v"(sac), "+v"(gac));
  bar_l();
  STAMP(0, 1);
#pragma unroll
  for (int p = 0; p < 7; p++) rd(0, p, 0);
  unsigned base = 0;
#pragma unroll 1
  for (int c = 0; c < 7; c++) {
    mm(0, true, base);
    asm volatile("s_barrier" ::: "memory");
    base = base == 4 * 32768 ? 0 : base + 32768;
    mm(1, true, base);
  }
  mm(0, true, base);
  mm(1, false, base);
  MF_PAD();
  STAMP(0, 9);
  {
    const float dg = (r & 2) ? ((r & 1) ? gac.w : gac.z) : ((r & 1) ? gac.y : gac.x);
    const float mean = sac.x * (1.0f / D_);
    const float rstd = __builtin_amdgcn_rsqf(fmaxf(dg * (1.0f / D_) - mean * mean, 0.f) + LN_EPS);
    if (c4 == q4) ldsw<f32x2>(98304 + (16 * hq + r) * 8, f32x2{mean, rstd});
  }
  bar_l();
  STAMP(0, 10);
  const int head = head0 + hq;
  const unsigned so = (l * 16) ^ ((hq & 1) * 256);
#pragma unroll
  for (int jp = 0; jp < 2; jp++) {
    const int rb = row0 / 32 + (jp ^ (hq >> 1));
    char* qd = ws + QF_OFF + (size_t)(head * 64 + rb) * 1024 + so;
    char* kd = ws + KV_OFF + (size_t)(head * 64 + rb) * 2048 + so;
    bf16x4 o[3][2];
#pragma unroll
    for (int e = 0; e < 2; e++) {
      const int j = 2 * jp + e;
      const unsigned fin = 98304 + (j ^ hq) * 128;
      const f32x2 st = ldsr<f32x2>(fin + r * 8);
      o[0][e] = pack4((acc[0][j] - st.x * cac[0]) * (st.y * QSCALE));
      o[1][e] = pack4((acc[1][j] - st.x * cac[1]) * st.y);
      const f32x4 s0 = ldsr<f32x4>(fin + 32 * c4), s1 = ldsr<f32x4>(fin + 32 * c4 + 16), a = acc[2][j];
      const float cc = cac[2].x;
      o[2][e] = pack4(f32x4{(a.x - s0.x * cc) * s0.y, (a.y - s0.z * cc) * s0.w, (a.z - s1.x * cc) * s1.y, (a.w - s1.z * cc) * s1.w});
    }
    *(bf16x8*)qd = swap_pair(o[0][0], o[0][1]);
    *(bf16x8*)kd = swap_pair(o[1][0], o[1][1]);
    *(bf16x8*)(kd + 1024) = swap_pair(o[2][0], o[2][1]);
  }
  STAMP(0, 11);
}

DEVI f32x16 pv(bf16x8 v0, bf16x8 v1, f32x16 p, f32x16 o) {
  f32x8 lo = __builtin_shufflevector(p, p, 0, 1, 2, 3, 4, 5, 6, 7), hi = __builtin_shufflevector(p, p, 8, 9, 10, 11, 12, 13, 14, 15);
  o = __builtin_amdgcn_smfmac_f32_32x32x32_bf16(v0, __builtin_bit_cast(bf16x16, lo), o, 0xDDDD, 0, 0);
  o = __builtin_amdgcn_smfmac_f32_32x32x32_bf16(v1, __builtin_bit_cast(bf16x16, hi), o, 0xDDDD, 0, 0);
  return o;
}
DEVI float max16(f32x16 s) {
  float m = fmaxf(fmaxf(s[0], s[1]), s[2]);
#pragma unroll
  for (int i = 3; i < 15; i += 2) m = fmaxf(fmaxf(m, s[i]), s[i + 1]);
  return max_x32(fmaxf(m, s[15]));
}
DEVI float mish(float v) {
  const float e = __builtin_amdgcn_exp2f(fminf(v * LOG2E, 40.f));
  const float n = e * (e + 2.0f);
  return v * n * __builtin_amdgcn_rcpf(n + 2.0f);
}
DEVI void signal(char* ws, int idx) {
  if (threadIdx.x == 256) __hip_atomic_fetch_add((GAS unsigned*)(ws + CNT_OFF) + idx * 64, 1u, __ATOMIC_RELAXED, __HIP_MEMORY_SCOPE_AGENT);
}
DEVI void await(char* ws, int idx, unsigned want) {
  if (threadIdx.x == 256) {
    for (unsigned spins = 0; spins < (1u << 20); spins++)
      if (__hip_atomic_load((GAS unsigned*)(ws + CNT_OFF) + idx * 64, __ATOMIC_RELAXED, __HIP_MEMORY_SCOPE_AGENT) >= want) break;
  }
}

#define EX(r) "v_exp_f32 v" #r ", v" #r "\n\t"
#define ATT_TILE(X0, X7, X8, X15, Y0, Y15, E1, E2, E3, VC0, VC1, VL0, VL1, OK, OV0, OV1, XTRA) \
  E1 "s_waitcnt lgkmcnt(0)\n\t" \
  "v_mfma_f32_32x32x16_bf16 v[" #Y0 ":" #Y15 "], %[k], %[q], %[c]\n\t" \
  "ds_read_b128 %[k], %[a] offset:" #OK "\n\t" \
  "s_mov_b64 exec, %[m]\n\t" \
  "ds_read_b128 %[" #VL0 "], %[a] offset:" #OV0 "\n\t" \
  "ds_read_b128 %[" #VL1 "], %[a] offset:" #OV1 "\n\t" \
  "s_mov_b64 exec, -1\n\t" \
  E2 "v_smfmac_f32_32x32x32_bf16 %[o], %[" #VC0 "], v[" #X0 ":" #X7 "], %[i]\n\t" XTRA \
  E3 "v_smfmac_f32_32x32x32_bf16 %[o], %[" #VC1 "], v[" #X8 ":" #X15 "], %[i]\n\t"
#define ATT_PAIR(O0, O1, O2, O3, O4, O5, XTRA) \
  ATT_TILE(96, 103, 104, 111, 112, 127, EX(98) EX(99) EX(100) EX(101), EX(102) EX(103) EX(104) EX(105), \
           EX(106) EX(107) EX(108) EX(109) EX(110) EX(111) EX(112) EX(113), va0, va1, vb0, vb1, O0, O1, O2, XTRA) \
  ATT_TILE(112, 119, 120, 127, 96, 111, EX(114) EX(115) EX(116) EX(117), EX(118) EX(119) EX(120) EX(121), \
           EX(122) EX(123) EX(124) EX(125) EX(126) EX(127) EX(96) EX(97), vb0, vb1, va0, va1, O3, O4, O5, XTRA)
#define ATT_DMA "global_load_lds_dwordx4 %[g], %[src]\n\tv_add_u32 %[g], 0x2000, %[g]\n\ts_add_u32 m0, m0, 0x2000\n\t"

constexpr size_t ST_OFF = 17u << 20;
#define PRIO_A 2
#define PRIO_B 3

__global__ void __launch_bounds__(512, 2) k_main(const __bf16* __restrict__ x, const __bf16* __restrict__ wo, const __bf16* __restrict__ w1,
                                                 const __bf16* __restrict__ b1, const __bf16* __restrict__ w2, char* __restrict__ ws, __bf16* __restrict__ out) {
  STAMP(1, 0);
  const int b = blockIdx.x, xcd = b & 7, i5 = b >> 3;
  const int tid = threadIdx.x, l = tid & 63, wv = __builtin_amdgcn_readfirstlane(tid >> 6);
  const int rt = i5 & 3, nt = i5 >> 2;
  const int rg = i5 & 1, ct = i5 >> 1;
  const int tt = wv & 3, kh = wv >> 2;
  const int lr = l >> 3, lc = l & 7;
  const unsigned wpo = lr * 1024 + (((((lc >> 1) ^ ((lr >> 1) & 3)) << 1) | (lc & 1)) << 4);
  {
    const int head = (xcd & 1) * 16 + (xcd >> 1) * 4 + (i5 & 3), rb = (i5 >> 2) * 8 + wv;
    bf16x8 q;
    gld16(q, ws + QF_OFF + (size_t)(head * 64 + rb) * 1024, l * 16);
    const char* src = ws + KV_OFF + (size_t)head * 131072 + wv * 1024;
#pragma unroll
    for (int p = 0; p < 4; p++) glds16(src + p * 8192, l * 16, p * 8192 + wv * 1024);
    const int r = l & 31;
    const unsigned cv = ((r & 27) == 16) ? 0x3F803F80u : 0u;
    bf16x8 va0 = __builtin_bit_cast(bf16x8, u32x4{cv, cv, cv, cv}), va1 = va0, vb0 = va0, vb1 = va0;
    unsigned la = l * 16, ga = l * 16 + 32768;
    const f32x16 zero = {};
    f32x16 o = zero;
    int idx = 0xDDDD;
    const unsigned line = (i5 * 8 + wv) * 64 + l;
    unsigned o1 = (line < 11008 ? line : 0) * 128, o2 = (line & 2047) * 128;
    asm volatile("s_waitcnt vmcnt(0)\n\ts_barrier" : "+v"(q), "+v"(o), "+v"(va0), "+v"(va1), "+v"(vb0), "+v"(vb1), "+v"(idx), "+v"(o1), "+v"(o2), "+v"(ga)::"memory");
    STAMP(1, 1);
    f32x16 sa = mfma32(ldsr16(la), q, zero), sb, cm;
    bf16x8 kf = ldsr16(la + 2048);
    if (r < 16) { va0 = ldsr16(la + 1024); va1 = ldsr16(la + 1280); }
    const float m0 = max16(sa);
#pragma unroll
    for (int i = 0; i < 16; i++) cm[i] = -m0;
    sa = sa + cm;
    sa[0] = __builtin_amdgcn_exp2f(sa[0]);
    sa[1] = __builtin_amdgcn_exp2f(sa[1]);
    const unsigned long long vmask = 0x0000FFFF0000FFFFull;
    unsigned keep;
    int n;
    asm volatile(
        "s_mov_b32 %[kp], m0\n\t"
        "s_mov_b32 m0, %[d0]\n\t"
        "s_mov_b32 %[n], 6\n\t"
        "s_cmp_eq_u32 %[kh], 0\n\t"
        "s_cbranch_scc1 1f\n\t"
        "s_setprio 1\n"
        "1:\n\t"
        ATT_PAIR(4096, 3072, 3328, 6144, 5120, 5376, ATT_DMA)
        "v_add_u32 %[a], 0x1000, %[a]\n\t"
        "s_sub_u32 %[n], %[n], 1\n\t"
        "s_cmp_gt_u32 %[n], %[pa]\n\t"
        "s_cbranch_scc1 1b\n\t"
        "s_setprio 0\n\t"
        "s_cmp_lg_u32 %[n], 0\n\t"
        "s_cbranch_scc1 1b\n\t"
        "v_add_u32 %[a], 0xfffff000, %[a]\n\t"
        "s_mov_b32 %[n], 13\n\t"
        "s_branch 3f\n"
        "2:\n\t"
        ATT_PAIR(4096, 3072, 3328, 6144, 5120, 5376, "")
        "3:\n\t"
        ATT_PAIR(8192, 7168, 7424, 10240, 9216, 9472, "")
        "v_add_u32 %[a], 0x2000, %[a]\n\t"
        "s_sub_u32 %[n], %[n], 1\n\t"
        "s_cmp_eq_u32 %[n], 12\n\t"
        "s_cbranch_scc1 4f\n\t"
        "s_cmp_gt_u32 %[n], %[pb]\n\t"
        "s_cbranch_scc1 2b\n\t"
        "s_setprio 0\n\t"
        "s_cmp_lg_u32 %[n], 0\n\t"
        "s_cbranch_scc1 2b\n\t"
        "s_branch 5f\n"
        "4:\n\t"
        "s_waitcnt vmcnt(0)\n\t"
        "s_barrier\n\t"
        "s_cmp_eq_u32 %[kh], 0\n\t"
        "s_cbranch_scc1 6f\n\t"
        "s_setprio 1\n"
        "6:\n\t"
        "global_load_lds_dwordx4 %[wp], %[wo0]\n\t"
        "s_add_u32 m0, m0, 0x2000\n\t"
        "v_add_u32 %[g], 0x10000, %[wp]\n\t"
        "global_load_lds_dwordx4 %[g], %[wo0]\n\t"
        "s_add_u32 m0, m0, 0x2000\n\t"
        "v_add_u32 %[g], 0x10000, %[g]\n\t"
        "global_load_lds_dwordx4 %[g], %[wo0]\n\t"
        "s_add_u32 m0, m0, 0x2000\n\t"
        "v_add_u32 %[g], 0x10000, %[g]\n\t"
        "global_load_lds_dwordx4 %[g], %[wo0]\n\t"
        "s_lshr_b32 m0, %[d0], 2\n\t"
        "s_sub_u32 m0, m0, 0x2000\n\t"
        "v_add_u32 %[a], 0xfffff000, %[a]\n\t"
        "global_load_lds_dword %[o1], %[w1]\n\t"
        "global_load_lds_dword %[o1], %[w2]\n\t"
        "global_load_lds_dword %[o2], %[wo1]\n\t"
        "s_mov_b32 %[n], 0x8000000d\n\t"
        "s_branch 7f\n"
        "5:\n\t"
        "s_branch 8f\n"
        "7:\n\t"
        ATT_PAIR(8192, 7168, 7424, 10240, 9216, 9472, "")
        "v_add_u32 %[a], 0x2000, %[a]\n\t"
        "s_mov_b32 %[n], 12\n\t"
        "s_branch 2b\n"
        "8:\n\t"
        "s_mov_b32 m0, %[kp]\n\t"
        "s_waitcnt lgkmcnt(0)\n\t"
        "s_nop 11"
        : [o] "+v"(o), [a] "+v"(la), [k] "+v"(kf), [va0] "+v"(va0), [va1] "+v"(va1), [vb0] "+v"(vb0), [vb1] "+v"(vb1),
          "+{v[96:111]}"(sa), "=&{v[112:127]}"(sb), [n] "=&s"(n), [kp] "=&s"(keep), [g] "+v"(ga)
        : [q] "v"(q), [c] "v"(cm), [i] "v"(idx), [m] "s"(vmask), [kh] "s"(kh), [d0] "s"(32768 + wv * 1024), [src] "s"(src), [pa] "i"(PRIO_A - 1), [pb] "i"(PRIO_B),
          [wp] "v"(wpo), [wo0] "s"((const char*)wo + wv * 8192 + nt * 128), [o1] "v"(o1), [o2] "v"(o2), [w1] "s"(w1), [w2] "s"(w2), [wo1] "s"((const char*)wo + 262144)
        : "memory", "scc");
    STAMP(1, 6);
    float lsum = o[8];
    float chk = 0.f;
#pragma unroll
    for (int i = 0; i < 8; i++) chk = fmaf(o[i], 0.f, chk);
    if (!__all(lsum < 1e18f && chk == 0.f)) {
      float m = -INFINITY;
      o = zero;
      const char* kv = ws + KV_OFF + (size_t)head * 131072 + l * 16;
#pragma unroll 1
      for (int t = 0; t < 64; t++) {
        const f32x16 sv = mfma32(*(const bf16x8*)(kv + t * 2048), q, zero);
        if (r < 16) { va0 = *(const bf16x8*)(kv + t * 2048 + 1024); va1 = *(const bf16x8*)(kv + t * 2048 + 1280); }
        const float mn = fmaxf(m, max16(sv));
        const float al = __builtin_amdgcn_exp2f(m - mn);
        m = mn;
        f32x16 p;
#pragma unroll
        for (int i = 0; i < 16; i++) p[i] = __builtin_amdgcn_exp2f(sv[i] - mn);
        o = pv(va0, va1, p, o * al);
      }
      lsum = o[8];
    }
    const float inv = __builtin_amdgcn_rcpf(lsum);
    bf16x8 ov;
#pragma unroll
    for (int i = 0; i < 8; i++) ov[i] = (__bf16)(o[i] * inv);
    gst16_sc1(ov, ws + OF_OFF + (size_t)(rb * 32 + head) * 1024, l * 16);
    wait_v_bar<0>();
    signal(ws, i5 >> 2);
    STAMP(1, 8);
  }
  const int r = l & 15, c4 = l >> 4, q4 = (l & 15) >> 2, p4 = l & 3;
  const int swp = ((c4 & 1) << 1) | (q4 >> 1);
  const int nun = ct < 14 ? 3 : 1;
  {
    const int t16 = xcd * 16 + rt * 4 + tt, pair = nt * 2 + kh;
    bf16x4 rx0, rx1;
    gld8(rx0, (const char*)x + (size_t)t16 * 16384 + pair * 64, r * 1024 + c4 * 8);
    gld8(rx1, (const char*)x + (size_t)t16 * 16384 + pair * 64 + 32, r * 1024 + c4 * 8);
    if (wv >= 6) {
      const int h2 = wv & 1;
#pragma unroll 1
      for (int p = 0; p < 16; p++) glds16((const char*)wo + (32 + p * 2 + h2) * 8192 + nt * 128, wpo, 98304 + (p * 2 + h2) * 1024);
      if (ct < 15) {
        unsigned wo1[3];
#pragma unroll
        for (int u = 0; u < 3; u++) {
          const int g = u * 64 + l, rr = g / 12, ch = g % 12;
          int sch = (((ch >> 1) ^ ((rr >> 2) & 1)) << 1) | (ch & 1);
          sch = sch < nun * 4 ? sch : (sch & 3);
          wo1[u] = rr * (F_ * 2) + sch * 16;
        }
#pragma unroll 1
        for (int it = 0; it < 16; it++)
#pragma unroll
          for (int u = 0; u < 3; u++) glds16((const char*)w1 + (it * 2 + h2) * 16 * (F_ * 2) + ct * 192, wo1[u], (it * 2 + h2) * 3072 + u * 1024);
        wait_v<48>();
      } else wait_v<0>();
    }
    unsigned wa[4];
#pragma unroll
    for (int j = 0; j < 4; j++) wa[j] = 131072 - kh * 32768 + (16 * (c4 >> 1) + 4 * (c4 & 1) + q4) * 128 + ((j ^ swp) << 5) + p4 * 8;
    f32x4 acc[4] = {};
    asm volatile("" : "+v"(acc[0]), "+v"(acc[1]), "+v"(acc[2]), "+v"(acc[3]));
    await(ws, xcd, 32);
    bar_l();
    STAMP(1, 9);
    const char* ab = ws + OF_OFF + (size_t)(t16 >> 1) * 32768 + (t16 & 1) * 256 + kh * 16384;
    const unsigned ao = (c4 >> 1) * 1024 + (c4 & 1) * 512 + r * 16;
    bf16x8 wf[8][4], af[8];
#pragma unroll
    for (int k = 0; k < 8; k++) gld16_sc1(af[k], ab + k * 2048, ao);
#pragma unroll
    for (int k = 0; k < 8; k++)
#pragma unroll
      for (int j = 0; j < 4; j++) wf[k][j] = cat8(trread(wa[j] + k * 4096), trread(wa[j] + k * 4096 + 1024));
    bar_l();
#pragma unroll
    for (int k = 0; k < 8; k++) {
      asm volatile("s_waitcnt vmcnt(%1)" : "+v"(af[k]) : "i"(7 - k) : "memory");
#pragma unroll
      for (int j = 0; j < 4; j++) MF(acc[j], wf[k][j], af[k]);
    }
    asm volatile("s_nop 7" : "+v"(rx0), "+v"(rx1), "+v"(acc[0]), "+v"(acc[1]), "+v"(acc[2]), "+v"(acc[3]));
    STAMP(1, 10);
    ldsw<f32x4>(131072 + wv * 2048 + l * 16, kh ? acc[0] : acc[2]);
    ldsw<f32x4>(131072 + wv * 2048 + 1024 + l * 16, kh ? acc[1] : acc[3]);
    bar_l();
    const f32x4 v0 = (kh ? acc[2] : acc[0]) + ldsr<f32x4>(131072 + (wv ^ 4) * 2048 + l * 16) + up4(rx0);
    const f32x4 v1 = (kh ? acc[3] : acc[1]) + ldsr<f32x4>(131072 + (wv ^ 4) * 2048 + 1024 + l * 16) + up4(rx1);
    const bf16x8 hres = cat8(pack4(v0), pack4(v1));
    gst16_sc1(hres, ws + HF_OFF + (size_t)(t16 * 16 + pair) * 1024, l * 16);
    const f32x4 h0 = up4(__builtin_shufflevector(hres, hres, 0, 1, 2, 3)), h1 = up4(__builtin_shufflevector(hres, hres, 4, 5, 6, 7));
    const f32x4 hs = h0 + h1, hq = h0 * h0 + h1 * h1;
    const f32x2 st = {red_c((hs.x + hs.y) + (hs.z + hs.w)), red_c((hq.x + hq.y) + (hq.z + hq.w))};
    if (c4 == 0) asm volatile("global_store_dwordx2 %0, %1, %2 sc1" ::"v"(r * 8), "v"(st), "s"(ws + ST_OFF + (size_t)(t16 * 16 + pair) * 128) : "memory");
    wait_v_bar<0>();
    signal(ws, 8 + xcd * 2 + (rt >> 1));
    STAMP(1, 11);
  }
  const char* w2b = (const char*)w2 + nt * 128 + (wv & 1) * 16384;
  auto w2load = [&](int ks, int slot) {
    glds16(w2b + ks * 32768, wpo, slot * 4096 + (wv & 1) * 2048);
    glds16(w2b + ks * 32768 + 8192, wpo, slot * 4096 + (wv & 1) * 2048 + 1024);
  };
  {
    const unsigned rowl = (4 * c4 + q4) * 192 + p4 * 8;
    const unsigned we = rowl + (c4 & 1) * 32, wod = rowl + 32 - (c4 & 1) * 32;
    if (wv >= 6) {
#pragma unroll 1
      for (int s = 0; s < 15; s++) w2load(s < 8 ? s : s + 14, s + 25);
    } else if (ct < 15) {
      const bf16x8 ones = __builtin_bit_cast(bf16x8, u32x4{0x3F803F80u, 0x3F803F80u, 0x3F803F80u, 0x3F803F80u});
      const unsigned a = ((wv & 1) ? wod : we) + (wv & 6) * 32;
      f32x4 c0 = {}, c1 = {};
#pragma unroll
      for (int k = 0; k < 16; k += 2) {
        c0 = mfma16(cat8(trread(a + k * 6144), trread(a + k * 6144 + 3072)), ones, c0);
        c1 = mfma16(cat8(trread(a + k * 6144 + 6144), trread(a + k * 6144 + 9216)), ones, c1);
      }
      if (r == 0) ldsw<f32x4>(98304 + wv * 64 + c4 * 16, c0 + c1);
    }
    if (ct < 15) {
      if (wv < 4) {
        bf16x8 wf[2][6], af[16][2];
        auto rd = [&](int k, int j) { const unsigned a = ((j & 1) ? wod : we) + (j & 6) * 32 + k * 6144; wf[k & 1][j] = cat8(trread(a), trread(a + 16 * 192)); SB0(); };
        bf16x4 bias[6];
#pragma unroll
        for (int j = 0; j < 6; j++) gld8(bias[j], (const char*)b1 + ct * 192 + (j < 2 * nun ? j : (j & 1)) * 32, c4 * 8);
#pragma unroll
        for (int j = 0; j < 6; j++) rd(0, j);
        f32x4 acc[2][6] = {};
        asm volatile("" : "+v"(acc[0][0]), "+v"(acc[0][1]), "+v"(acc[0][2]), "+v"(acc[0][3]), "+v"(acc[0][4]), "+v"(acc[0][5]),
                     "+v"(acc[1][0]), "+v"(acc[1][1]), "+v"(acc[1][2]), "+v"(acc[1][3]), "+v"(acc[1][4]), "+v"(acc[1][5]));
        bar_l();
        STAMP(1, 12);
        const int u16 = xcd * 16 + rg * 8 + wv * 2;
        const char* ab = ws + HF_OFF + (size_t)u16 * 16384;
        constexpr int WIN = 6;
#pragma unroll
        for (int k = 0; k < WIN; k++) { gld16_sc1(af[k][0], ab + k * 1024, l * 16); gld16_sc1(af[k][1], ab + 16384 + k * 1024, l * 16); }
        f32x2 sp[2][4];
#pragma unroll
        for (int k = 0; k < 16; k++) {
          asm volatile("s_waitcnt vmcnt(%2)" : "+v"(af[k][0]), "+v"(af[k][1]) : "i"(k + WIN <= 16 ? 2 * WIN - 2 : k < 14 ? 2 * WIN - 2 : k == 14 ? 10 : 8) : "memory");
#pragma unroll
          for (int j = 0; j < 6; j++) {
            MF(acc[0][j], wf[k & 1][j], af[k][0]);
            if (k < 15) rd(k + 1, j);
            MF(acc[1][j], wf[k & 1][j], af[k][1]);
            if (j < 2) {
              if (k + WIN < 16) gld16_sc1(af[k + WIN][j], ab + j * 16384 + (k + WIN) * 1024, l * 16);
              else if (k + WIN < 20) asm volatile("global_load_dwordx2 %0, %1, %2 sc1" : "=v"(sp[j][k + WIN - 16]) : "v"(c4 * 512 + r * 8), "s"(ws + ST_OFF + (size_t)(u16 + j) * 2048 + (k + WIN - 16) * 128) : "memory");
            }
          }
        }
        asm volatile("s_waitcnt vmcnt(0)\n\ts_nop 7" : "+v"(bias[0]), "+v"(bias[1]), "+v"(bias[2]), "+v"(bias[3]), "+v"(bias[4]), "+v"(bias[5]),
                     "+v"(sp[0][0]), "+v"(sp[0][1]), "+v"(sp[0][2]), "+v"(sp[0][3]), "+v"(sp[1][0]), "+v"(sp[1][1]), "+v"(sp[1][2]), "+v"(sp[1][3]),
                     "+v"(acc[0][0]), "+v"(acc[0][1]), "+v"(acc[0][2]), "+v"(acc[0][3]), "+v"(acc[0][4]), "+v"(acc[0][5]),
                     "+v"(acc[1][0]), "+v"(acc[1][1]), "+v"(acc[1][2]), "+v"(acc[1][3]), "+v"(acc[1][4]), "+v"(acc[1][5])::"memory");
        asm volatile("s_barrier" ::: "memory");
        STAMP(1, 13);
        float rstd[2], mr[2];
#pragma unroll
        for (int t = 0; t < 2; t++) {
          const f32x2 s2 = (sp[t][0] + sp[t][1]) + (sp[t][2] + sp[t][3]);
          const float mean = red_c(s2.x) * (1.0f / D_);
          rstd[t] = __builtin_amdgcn_rsqf(fmaxf(red_c(s2.y) * (1.0f / D_) - mean * mean, 0.f) + LN_EPS);
          mr[t] = mean * rstd[t];
        }
#pragma unroll
        for (int u = 0; u < 3; u++) {
          if (u < nun) {
            const f32x4 cs0 = ldsr<f32x4>(98304 + u * 128 + 16 * c4), cs1 = ldsr<f32x4>(98304 + u * 128 + 64 + 16 * c4);
            const f32x4 b0 = up4(bias[2 * u]), b1v = up4(bias[2 * u + 1]);
#pragma unroll
            for (int t = 0; t < 2; t++) {
              const f32x4 v0 = acc[t][2 * u] * rstd[t] + (b0 - mr[t] * cs0), v1 = acc[t][2 * u + 1] * rstd[t] + (b1v - mr[t] * cs1);
              gst16_sc1(cat8(pack4(f32x4{mish(v0.x), mish(v0.y), mish(v0.z), mish(v0.w)}), pack4(f32x4{mish(v1.x), mish(v1.y), mish(v1.z), mish(v1.w)})),
                        ws + AF_OFF + (size_t)((u16 + t) * 43 + ct * 3 + u) * 1024, l * 16);
            }
          }
        }
      } else {
        await(ws, 8 + xcd * 2 + rg, 16);
        bar_l();
        asm volatile("s_barrier" ::: "memory");
      }
    }
    if (wv >= 6) {
#pragma unroll 1
      for (int s = 0; s < 24; s++) w2load(s < 12 ? s + 8 : s + 17, s);
      wait_v<48>();
      asm volatile("s_barrier" ::: "memory");
    } else wait_v_bar<0>();
    if (ct < 15) signal(ws, 24 + xcd * 2 + rg);
    STAMP(1, 14);
  }
  {
    const int tp = wv & 1, k2 = (wv >> 1) & 1;
    if (wv < 4) {
      const int t16 = xcd * 16 + rt * 4 + tp * 2;
      unsigned wa[4];
#pragma unroll
      for (int j = 0; j < 4; j++) wa[j] = (4 * c4 + q4) * 128 + ((j ^ swp) << 5) + p4 * 8;
      bf16x8 wf[3][4], af[22][2], hres[2];
      const unsigned s0 = (25 + k2 * 8) * 4096, s1 = k2 * 13 * 4096;
      auto rd = [&](int k, int j) {
        const unsigned off = k < 7 ? s0 + k * 4096 : k == 7 ? (k2 ? s1 - 4096 : s0 + k * 4096) : k < 19 ? s1 + (k - 8) * 4096 : k == 19 ? (k2 ? s0 : s1 + 11 * 4096) : s0 + (k - 20 + k2) * 4096;
        wf[k % 3][j] = cat8(trread(wa[j] + off), trread(wa[j] + off + 2048));
        SB0();
      };
#pragma unroll
      for (int j = 0; j < 8; j++) rd(j >> 2, j & 3);
      gld16_sc1(hres[0], ws + HF_OFF + (size_t)((t16 + k2) * 16 + nt * 2) * 1024, l * 16);
      gld16_sc1(hres[1], ws + HF_OFF + (size_t)((t16 + k2) * 16 + nt * 2 + 1) * 1024, l * 16);
      f32x4 acc[2][4] = {};
      asm volatile("" : "+v"(acc[0][0]), "+v"(acc[0][1]), "+v"(acc[0][2]), "+v"(acc[0][3]), "+v"(acc[1][0]), "+v"(acc[1][1]), "+v"(acc[1][2]), "+v"(acc[1][3]));
      bar_l();
      asm volatile("s_barrier" ::: "memory");
      STAMP(1, 2);
      const char* ab0 = ws + AF_OFF + (size_t)((t16 + k2) * 43 + k2 * 22) * 1024;
      const char* ab1 = ws + AF_OFF + (size_t)((t16 + (k2 ^ 1)) * 43 + k2 * 22) * 1024;
      constexpr int WIN = 8;
#pragma unroll
      for (int k = 0; k < WIN; k++) { gld16_sc1(af[k][0], ab0 + k * 1024, l * 16); gld16_sc1(af[k][1], ab1 + k * 1024, l * 16); }
#pragma unroll
      for (int k = 0; k < 22; k++) {
        asm volatile("s_waitcnt vmcnt(%2)" : "+v"(af[k][0]), "+v"(af[k][1]) : "i"(k + WIN <= 22 ? 2 * WIN - 2 : 2 * (21 - k)) : "memory");
        if (k < 21 || !k2) {
#pragma unroll
          for (int j = 0; j < 4; j++) {
            MF(acc[0][j], wf[k % 3][j], af[k][0]);
            if (k < 20) rd(k + 2, j);
            MF(acc[1][j], wf[k % 3][j], af[k][1]);
            if (j < 2 && k + WIN < 22) gld16_sc1(af[k + WIN][j], (j ? ab1 : ab0) + (k + WIN < 21 ? k + WIN : 21 - k2) * 1024, l * 16);
          }
        }
      }
      asm volatile("s_nop 7\n\ts_barrier" : "+v"(hres[0]), "+v"(hres[1]), "+v"(acc[0][0]), "+v"(acc[0][1]), "+v"(acc[0][2]), "+v"(acc[0][3]),
                   "+v"(acc[1][0]), "+v"(acc[1][1]), "+v"(acc[1][2]), "+v"(acc[1][3])::"memory");
      STAMP(1, 3);
#pragma unroll
      for (int j = 0; j < 4; j++) ldsw<f32x4>(wv * 4096 + j * 1024 + l * 16, acc[1][j]);
      bar_l();
      char* op = (char*)out + (size_t)((t16 + k2) * 16 + r) * 1024 + nt * 128 + c4 * 8;
#pragma unroll
      for (int j = 0; j < 4; j++) {
        const bf16x8 hh = hres[j >> 1];
        const f32x4 v = acc[0][j] + ldsr<f32x4>((wv ^ 2) * 4096 + j * 1024 + l * 16) + ((j & 1) ? up4(__builtin_shufflevector(hh, hh, 4, 5, 6, 7)) : up4(__builtin_shufflevector(hh, hh, 0, 1, 2, 3)));
        *(bf16x4*)(op + j * 32) = pack4(v);
      }
      STAMP(1, 4);
    } else {
      asm volatile("s_barrier" ::: "memory");
      if (wv >= 6) {
#pragma unroll
        for (int s = 0; s < 4; s++) w2load(s < 2 ? s + 20 : s + 39, s < 2 ? s + 25 : s + 31);
      }
      await(ws, 24 + xcd * 2 + (rt >> 1), 15);
      wait_v_bar<0>();
    }
  }
}

extern "C" void kernel_launch(void* const* d_in, const int* in_sizes, int n_in, void* d_out, int out_size, void* d_ws, size_t ws_size, hipStream_t stream) {
  const __bf16* x = (const __bf16*)d_in[0];
  const __bf16* wqkv = (const __bf16*)d_in[1];
  const __bf16* wo = (const __bf16*)d_in[2];
  const __bf16* w1 = (const __bf16*)d_in[4];
  const __bf16* b1 = (const __bf16*)d_in[5];
  const __bf16* w2 = (const __bf16*)d_in[6];
  char* ws = (char*)d_ws;
  (void)hipFuncSetAttribute((const void*)k_qkv, hipFuncAttributeMaxDynamicSharedMemorySize, LDS_BYTES);
  (void)hipFuncSetAttribute((const void*)k_main, hipFuncAttributeMaxDynamicSharedMemorySize, LDS_BYTES);
  k_qkv<<<256, 512, LDS_BYTES, stream>>>(x, wqkv, ws);
  k_main<<<256, 512, LDS_BYTES, stream>>>(x, wo, w1, b1, w2, ws, (__bf16*)d_out);
}
```

```cpp
#include <hip/hip_runtime.h>
#include <hip/hip_bf16.h>

typedef __bf16 bf16x4 __attribute__((ext_vector_type(4)));
typedef __bf16 bf16x8 __attribute__((ext_vector_type(8)));
typedef __bf16 bf16x16 __attribute__((ext_vector_type(16)));
typedef float f32x2 __attribute__((ext_vector_type(2)));
typedef float f32x4 __attribute__((ext_vector_type(4)));
typedef float f32x8 __attribute__((ext_vector_type(8)));
typedef float f32x16 __attribute__((ext_vector_type(16)));
typedef unsigned u32x2 __attribute__((ext_vector_type(2)));
typedef unsigned u32x4 __attribute__((ext_vector_type(4)));
#define LAS __attribute__((address_space(3)))
#define GAS __attribute__((address_space(1)))
#define DEVI __device__ __forceinline__

constexpr int D_ = 512, F_ = 1376;
constexpr size_t QF_OFF = 0, KV_OFF = 2u << 20, OF_OFF = 6u << 20, HF_OFF = 8u << 20, AF_OFF = 10u << 20;
constexpr size_t ST_OFF = AF_OFF + 128 * 43 * 1024;
constexpr size_t EP_OFF = 16u << 20, PF_OFF = EP_OFF + 4096, KF_OFF = EP_OFF + 8192;
static_assert(ST_OFF + 131072 <= OF_OFF + 256 * 40960 && OF_OFF + 256 * 40960 <= EP_OFF, "poisoned range");
constexpr float LN_EPS = 1e-5f;
constexpr float LOG2E = 1.4426950408889634f;
constexpr float QSCALE = 0.25f * LOG2E;
constexpr int LDS_BYTES = 163840;

#define SB0() __builtin_amdgcn_sched_barrier(0)
template <int V> struct IC { static constexpr int v = V; };
template <int I, int N, class F> __device__ __forceinline__ void static_for(F&& f) { if constexpr (I < N) { f(IC<I>{}); static_for<I + 1, N>(f); } }
template <int N> DEVI void wait_v() { asm volatile("s_waitcnt vmcnt(%0)" ::"i"(N) : "memory"); }
DEVI void bar_l() { asm volatile("s_waitcnt lgkmcnt(0)\n\ts_barrier" ::: "memory"); }

DEVI void glds16_sc1(const void* base, unsigned voff, unsigned dst) {
  unsigned keep;
  asm volatile("s_mov_b32 %0, m0\n\ts_mov_b32 m0, %3\n\ts_nop 0\n\tglobal_load_lds_dwordx4 %1, %2 sc1\n\ts_mov_b32 m0, %0"
               : "=&s"(keep) : "v"(voff), "s"(base), "s"(dst) : "memory");
}
template <int N, int GS, int DS> DEVI void glds_seq(const void* base, unsigned voff, unsigned dst) {
  unsigned keep;
  asm volatile("s_mov_b32 %0, m0\n\ts_mov_b32 m0, %3\n\ts_nop 0\n\t.rept %c4\n\tglobal_load_lds_dwordx4 %1, %2\n\ts_add_u32 m0, m0, %c6\n\tv_add_u32 %1, %c5, %1\n\t.endr\n\ts_mov_b32 m0, %0"
               : "=&s"(keep), "+v"(voff) : "s"(base), "s"(dst), "i"(N), "i"(GS), "i"(DS) : "memory", "scc");
}
template <int N> DEVI void wait4(bf16x8& a, bf16x8& b, bf16x8& c, bf16x8& d) { asm volatile("s_waitcnt vmcnt(%4)" : "+v"(a), "+v"(b), "+v"(c), "+v"(d) : "i"(N) : "memory"); }
template <int N> DEVI void wait_l() { __builtin_amdgcn_s_waitcnt(0xC07F | (N << 8)); }
template <int OFF> DEVI void gld16o_sc1(bf16x8& d, const void* base, unsigned voff) { asm volatile("global_load_dwordx4 %0, %1, %2 offset:%3 sc1" : "=v"(d) : "v"(voff), "s"(base), "i"(OFF) : "memory"); }
template <int I> DEVI void frag_sc1(bf16x8& d, const char* base, unsigned voff) { gld16o_sc1<((I & 7) - 4) * 1024>(d, base + (I & ~7) * 1024 + 4096, voff); }
DEVI void gld16(bf16x8& d, const void* base, unsigned voff) { asm volatile("global_load_dwordx4 %0, %1, %2" : "=v"(d) : "v"(voff), "s"(base) : "memory"); }
DEVI void gld16_sc1(bf16x8& d, const void* base, unsigned voff) { asm volatile("global_load_dwordx4 %0, %1, %2 sc1" : "=v"(d) : "v"(voff), "s"(base) : "memory"); }
DEVI void gld4_sc1(unsigned& d, const void* base, unsigned voff) { asm volatile("global_load_dword %0, %1, %2 sc1" : "=v"(d) : "v"(voff), "s"(base) : "memory"); }
DEVI void gld8(bf16x4& d, const void* base, unsigned voff) { asm volatile("global_load_dwordx2 %0, %1, %2" : "=v"(d) : "v"(voff), "s"(base) : "memory"); }
DEVI void gst16_sc1(bf16x8 d, void* base, unsigned voff) { asm volatile("global_store_dwordx4 %0, %1, %2 sc1\n\ts_nop 1" ::"v"(voff), "v"(d), "s"(base) : "memory"); }

DEVI bf16x4 trread(unsigned a) { return __builtin_amdgcn_ds_read_tr16_b64_v4bf16((LAS bf16x4*)(size_t)a); }
DEVI bf16x8 ldsr16(unsigned a) { return *(const LAS bf16x8*)(size_t)a; }
template <class T> DEVI T ldsr(unsigned a) { return *(const LAS T*)(size_t)a; }
template <class T> DEVI void ldsw(unsigned a, T v) { *(LAS T*)(size_t)a = v; }
DEVI bf16x8 cat8(bf16x4 a, bf16x4 b) { return __builtin_shufflevector(a, b, 0, 1, 2, 3, 4, 5, 6, 7); }
DEVI f32x4 mfma16(bf16x8 a, bf16x8 b, f32x4 c) { return __builtin_amdgcn_mfma_f32_16x16x32_bf16(a, b, c, 0, 0, 0); }
DEVI void MF(f32x4& c, const bf16x8& a, const bf16x8& b) { asm volatile("v_mfma_f32_16x16x32_bf16 %0, %1, %2, %0" : "+v"(c) : "v"(a), "v"(b)); }
DEVI f32x16 mfma32(bf16x8 a, bf16x8 b, f32x16 c) { return __builtin_amdgcn_mfma_f32_32x32x16_bf16(a, b, c, 0, 0, 0); }
DEVI float add_x32(float v) { auto r = __builtin_amdgcn_permlane32_swap(__float_as_uint(v), __float_as_uint(v), false, false); return __uint_as_float(r[0]) + __uint_as_float(r[1]); }
DEVI float add_x16(float v) { auto r = __builtin_amdgcn_permlane16_swap(__float_as_uint(v), __float_as_uint(v), false, false); return __uint_as_float(r[0]) + __uint_as_float(r[1]); }
DEVI float max_x32(float v) { auto r = __builtin_amdgcn_permlane32_swap(__float_as_uint(v), __float_as_uint(v), false, false); return fmaxf(__uint_as_float(r[0]), __uint_as_float(r[1])); }
DEVI float red_c(float v) { return add_x32(add_x16(v)); }
DEVI bf16x4 pack4(f32x4 v) { return bf16x4{(__bf16)v.x, (__bf16)v.y, (__bf16)v.z, (__bf16)v.w}; }
DEVI f32x4 up4(bf16x4 v) { return f32x4{(float)v[0], (float)v[1], (float)v[2], (float)v[3]}; }

DEVI bf16x8 swap_pair(bf16x4 a, bf16x4 b) {
  const u32x2 x = __builtin_bit_cast(u32x2, a), y = __builtin_bit_cast(u32x2, b);
  const auto s0 = __builtin_amdgcn_permlane16_swap(x[0], y[0], false, false), s1 = __builtin_amdgcn_permlane16_swap(x[1], y[1], false, false);
  return __builtin_bit_cast(bf16x8, u32x4{s0[0], s1[0], s0[1], s1[1]});
}

DEVI f32x16 pv(bf16x8 v0, bf16x8 v1, f32x16 p, f32x16 o) {
  f32x8 lo = __builtin_shufflevector(p, p, 0, 1, 2, 3, 4, 5, 6, 7), hi = __builtin_shufflevector(p, p, 8, 9, 10, 11, 12, 13, 14, 15);
  o = __builtin_amdgcn_smfmac_f32_32x32x32_bf16(v0, __builtin_bit_cast(bf16x16, lo), o, 0xDDDD, 0, 0);
  o = __builtin_amdgcn_smfmac_f32_32x32x32_bf16(v1, __builtin_bit_cast(bf16x16, hi), o, 0xDDDD, 0, 0);
  return o;
}
DEVI float max16(f32x16 s) {
  float m = fmaxf(fmaxf(s[0], s[1]), s[2]);
#pragma unroll
  for (int i = 3; i < 15; i += 2) m = fmaxf(fmaxf(m, s[i]), s[i + 1]);
  return max_x32(fmaxf(m, s[15]));
}
template <int N> DEVI void mish_n(f32x4 (&t)[N]) {
  f32x4 e[N], n[N];
#pragma unroll
  for (int k = 0; k < N; k++) e[k] = t[k] - 16.0f;
  SB0();
#pragma unroll
  for (int k = 0; k < N; k++)
#pragma unroll
    for (int i = 0; i < 4; i++) e[k][i] = __builtin_amdgcn_fmed3f(__builtin_amdgcn_exp2f(e[k][i]), 0.f, 1.f);
  SB0();
#pragma unroll
  for (int k = 0; k < N; k++) n[k] = e[k] + 0x1p-15f;
  SB0();
#pragma unroll
  for (int k = 0; k < N; k++) n[k] = n[k] * e[k];
  SB0();
#pragma unroll
  for (int k = 0; k < N; k++) { e[k] = n[k] * LOG2E + (0x1p-31f * LOG2E); t[k] = t[k] * n[k]; }
  SB0();
#pragma unroll
  for (int k = 0; k < N; k++)
#pragma unroll
    for (int i = 0; i < 4; i++) e[k][i] = __builtin_amdgcn_rcpf(e[k][i]);
  SB0();
#pragma unroll
  for (int k = 0; k < N; k++) t[k] = t[k] * e[k];
}

constexpr unsigned POISON = 0xFFFFFFFFu;
constexpr unsigned SPIN_MAX = 1u << 16;
DEVI unsigned mx4(const bf16x8& f) { const u32x4 u = __builtin_bit_cast(u32x4, f); return max(max(u.x, u.y), max(u.z, u.w)); }
DEVI bool any_nan(float v) { return __builtin_amdgcn_ballot_w64(v != v) != 0; }
DEVI bool any_poison(unsigned m) { return __builtin_amdgcn_ballot_w64(m == POISON) != 0; }
DEVI void hint_spin(const void* base, unsigned voff) {
  for (unsigned s = 0; s < SPIN_MAX; s++) {
    unsigned v;
    asm volatile("global_load_dword %0, %1, %2 sc1\n\ts_waitcnt vmcnt(0)" : "=&v"(v) : "v"(voff), "s"(base) : "memory");
    if (!any_poison(v)) break;
    __builtin_amdgcn_s_sleep(2);
  }
}
DEVI void refetch(bf16x8& f, const void* base, unsigned voff) {
  for (unsigned s = 0; s < SPIN_MAX; s++) {
    asm volatile("global_load_dwordx4 %0, %1, %2 sc1\n\ts_waitcnt vmcnt(0)" : "=&v"(f) : "v"(voff), "s"(base) : "memory");
    if (!any_poison(mx4(f))) break;
  }
}
template <int GAP> DEVI void flag_spin3(const void* base, unsigned voff, unsigned long long tag, u32x2 (&j)[3]) {
  unsigned n = SPIN_MAX;
  asm volatile("global_load_dwordx2 %0, %4, %5 sc1\n\ts_sleep %c6\n\tglobal_load_dwordx2 %1, %4, %5 sc1\n\ts_sleep %c6\n"
               "1:\n\tglobal_load_dwordx2 %2, %4, %5 sc1\n\ts_waitcnt vmcnt(2)\n\tv_cmp_ne_u64 vcc, %7, %0\n\ts_cbranch_vccz 2f\n\t"
               "global_load_dwordx2 %0, %4, %5 sc1\n\ts_waitcnt vmcnt(2)\n\tv_cmp_ne_u64 vcc, %7, %1\n\ts_cbranch_vccz 2f\n\t"
               "global_load_dwordx2 %1, %4, %5 sc1\n\ts_waitcnt vmcnt(2)\n\tv_cmp_ne_u64 vcc, %7, %2\n\ts_sub_u32 %3, %3, 1\n\ts_cbranch_vccz 2f\n\ts_cbranch_scc0 1b\n2:"
               : "=&v"(j[0]), "=&v"(j[1]), "=&v"(j[2]), "+s"(n) : "v"(voff), "s"(base), "i"(GAP), "s"(tag) : "memory", "vcc", "scc");
}
#define UNLIKELY(c) __builtin_expect(!!(c), 0)

#define EX(r) "v_exp_f32 v" #r ", v" #r "\n\t"
#define QK(Y0, Y15, Q, C) "v_mfma_f32_32x32x16_bf16 v[" #Y0 ":" #Y15 "], %[k], %[" #Q "], %[" #C "]\n\t"
#define PV(O, V, X0, X7) "v_smfmac_f32_32x32x32_bf16 %[" #O "], %[" #V "], v[" #X0 ":" #X7 "], %[i]\n\t"
#define ATT_KT(VC0, VC1, VL0, VL1, OK, OV0, OV1, XTRA, XTRB) \
  EX(98) EX(99) EX(100) EX(101) QK(112, 127, q1, c1) \
  "ds_read_b128 %[k], %[a] offset:" #OK "\n\t" \
  "s_mov_b64 exec, %[m]\n\t" \
  "ds_read_b128 %[" #VL0 "], %[a] offset:" #OV0 "\n\t" \
  "ds_read_b128 %[" #VL1 "], %[a] offset:" #OV1 "\n\t" \
  "s_mov_b64 exec, -1\n\t" \
  EX(102) EX(103) EX(104) EX(105) PV(o0, VC0, 96, 103) XTRA \
  EX(106) EX(107) EX(108) EX(109) EX(110) EX(111) EX(112) EX(113) PV(o0, VC1, 104, 111) \
  EX(114) EX(115) EX(116) "s_waitcnt lgkmcnt(0)\n\t" QK(96, 111, q0, c0) \
  EX(117) EX(118) EX(119) EX(120) EX(121) PV(o1, VC0, 112, 119) XTRB \
  EX(122) EX(123) EX(124) EX(125) EX(126) EX(127) EX(96) EX(97) PV(o1, VC1, 120, 127)
#define ATT_DMAX(POL) "global_load_lds_dwordx4 %[g], %[src]" POL "\n\ts_add_u32 m0, m0, %[ds]\n\tv_add_u32 %[g], %[gs], %[g]\n\t"
#define ATT_DMA ATT_DMAX("")
#define ATT_DMS ATT_DMAX(" sc1")
#define ATT_HEAD "s_cmp_eq_u32 %[kh], 0\n\ts_cbranch_scc1 1f\n\ts_setprio 1\n"
#define ATT_ITER(X0, X1, X2, X3) "1:\n\t" \
      ATT_KT(va0, va1, vb0, vb1, 4096, 5120, 5376, X0, X1) ATT_KT(vb0, vb1, va0, va1, 8192, 9216, 9472, X2, X3) \
      "v_add_u32 %[a], 0x2000, %[a]\n\ts_sub_u32 %[n], %[n], 1\n\t"
#define ATT_TAIL "s_cmp_gt_u32 %[n], %[ps]\n\ts_cbranch_scc1 1b\n\ts_setprio 0\n\ts_cmp_lg_u32 %[n], 0\n\ts_cbranch_scc1 1b\n\t"
#define ATT_BODY(X0, X1, X2, X3) ATT_HEAD ATT_ITER(X0, X1, X2, X3) ATT_TAIL
#define ATT_PZ "s_mov_b64 exec, %[pm]\n\tglobal_store_dwordx4 %[po], %[pz], %[pb] sc1\n\ts_mov_b64 exec, -1\n\tv_add_u32 %[po], 0x280, %[po]\n\t"
#define ATT_OUT [o0] "+v"(o0), [o1] "+v"(o1), [a] "+v"(la), [k] "+v"(kf), [va0] "+v"(va0), [va1] "+v"(va1), [vb0] "+v"(vb0), [vb1] "+v"(vb1), \
        "+{v[96:111]}"(sa), "=&{v[112:127]}"(sb), [n] "+s"(n)
#define ATT_IN(PS) [q0] "v"(q0), [q1] "v"(q1), [c0] "v"(c0), [c1] "v"(c1), [i] "v"(idx), [m] "s"(vmask), [kh] "s"(kh), [ps] "i"(PS)
constexpr int K_SLEEP = 36, H_SLEEP = 10, A_SLEEP = 8, P3_GAP = 5, PARK_SPLIT = 14, PARK_SLEEP = 24, PS_A = 1, PS_B = 2, PS_C = 1;

__global__ void __launch_bounds__(512, 2) k_block(const __bf16* __restrict__ x, const __bf16* __restrict__ w, const __bf16* __restrict__ wo, const __bf16* __restrict__ w1,
                                                  const __bf16* __restrict__ b1, const __bf16* __restrict__ w2, char* __restrict__ ws, __bf16* __restrict__ out) {
  const int b = blockIdx.x, xcd = b & 7, i5 = b >> 3;
  const int tid = threadIdx.x, l = tid & 63, wv = __builtin_amdgcn_readfirstlane(tid >> 6);
  const int tt = wv & 3, kh = wv >> 2;
  const int lr = l >> 3, lc = l & 7;
  const int r = l & 15, c4 = l >> 4, q4 = (l & 15) >> 2, p4 = l & 3;
  u32x2 tag;
  {
    const int row0 = (xcd >> 1) * 512 + (i5 & 7) * 64, head0 = (xcd & 1) * 16 + (i5 >> 3) * 4;
    constexpr unsigned RDY = 1024, DONE = 1056;
    if (kh) {
      unsigned xoff[2], woff[2];
#pragma unroll
      for (int u = 0; u < 2; u++) {
        const int xrow = 16 * tt + 8 * u + lr;
        xoff[u] = xrow * 1024 + ((lc ^ ((xrow >> 1) & 7)) << 4);
        woff[u] = xrow * 3072 + (((((lc >> 1) ^ (((xrow >> 1) & 1) | ((xrow >> 2) & 2))) << 1) | (lc & 1)) << 4);
      }
      const char* xb = (const char*)x + (size_t)row0 * 1024;
      const char* wb = (const char*)w + head0 * 32;
      const int rot = i5 & 7;
      auto stage = [&](int c) {
        unsigned keep;
        asm volatile("s_mov_b32 %0, m0\n\ts_mov_b32 m0, %7\n\ts_nop 0\n\t"
                     "global_load_lds_dwordx4 %1, %5\n\ts_add_u32 m0, m0, 0x400\n\ts_nop 0\n\tglobal_load_lds_dwordx4 %2, %5\n\ts_add_u32 m0, m0, 0x1c00\n\ts_nop 0\n\t"
                     "global_load_lds_dwordx4 %3, %6\n\ts_add_u32 m0, m0, 0x400\n\ts_nop 0\n\tglobal_load_lds_dwordx4 %4, %6\n\ts_add_u32 m0, m0, 0x1800\n\ts_nop 0\n\t"
                     "global_load_lds_dwordx4 %3, %6 offset:1024\n\ts_add_u32 m0, m0, 0x400\n\ts_nop 0\n\tglobal_load_lds_dwordx4 %4, %6 offset:1024\n\ts_add_u32 m0, m0, 0x1800\n\ts_nop 0\n\t"
                     "global_load_lds_dwordx4 %3, %6 offset:2048\n\ts_add_u32 m0, m0, 0x400\n\ts_nop 0\n\tglobal_load_lds_dwordx4 %4, %6 offset:2048\n\ts_mov_b32 m0, %0"
                     : "=&s"(keep) : "v"(xoff[0]), "v"(xoff[1]), "v"(woff[0]), "v"(woff[1]), "s"(xb + ((c + rot) & 7) * 128), "s"(wb + ((c + rot) & 7) * 64 * 3072),
                       "s"(32768 + (c & 3) * 32768 + tt * 2048) : "memory", "scc");
      };
      unsigned tj;
      asm volatile("global_load_dword %0, %1, %2" : "=v"(tj) : "v"(((i5 >> 3) * 16 + tt * 4 + (lr & 3)) * 1024 + lc * 128), "s"(xb) : "memory");
      stage(0); stage(1);
      asm volatile("s_barrier" ::: "memory");
      wait_v<8>(); *(volatile LAS unsigned*)(size_t)(RDY + tt * 4) = 1;
      stage(2);
      wait_v<8>(); *(volatile LAS unsigned*)(size_t)(RDY + tt * 4) = 2;
      stage(3);
      wait_v<8>(); *(volatile LAS unsigned*)(size_t)(RDY + tt * 4) = 3;
#pragma unroll 1
      for (int c = 4; c < 8; c++) {
#pragma unroll 1
        for (unsigned s = 0; s < SPIN_MAX; s++) {
          const u32x4 d = *(const volatile LAS u32x4*)(size_t)DONE;
          if (__builtin_amdgcn_readfirstlane(min(min(d.x, d.y), min(d.z, d.w))) > (unsigned)(c - 4)) break;
          wait_v<0>();
          *(volatile LAS unsigned*)(size_t)(RDY + tt * 4) = c;
          __builtin_amdgcn_s_sleep(1);
        }
        asm volatile("" ::: "memory");
        stage(c);
        wait_v<8>();
        *(volatile LAS unsigned*)(size_t)(RDY + tt * 4) = c;
      }
      asm volatile("s_waitcnt vmcnt(0)" : "+v"(tj)::"memory");
      *(volatile LAS unsigned*)(size_t)(RDY + tt * 4) = 8;
      {
        const int id = b * 4 + tt, ln = min(id * 11 + (l < 11 ? l : l < 22 ? l - 11 : 0), 11007);
        const char* tp = l < 11 ? (const char*)w1 + ln * 128 : l < 22 ? (const char*)w2 + ln * 128 : (const char*)wo + (id * 4 + (l & 3)) * 128;
        asm volatile("global_load_dwordx2 %0, %2, %3 sc1\n\tglobal_load_dword %1, %4, off" : "=&v"(tag), "=&v"(tj) : "v"(0), "s"(ws + EP_OFF), "v"(tp) : "memory");
      }
      bar_l();
      asm volatile("s_waitcnt vmcnt(0)" : "+v"(tag), "+v"(tj)::"memory");
    } else {
      if (l < 16) ldsw<unsigned>(RDY + l * 4, 0u);
      bar_l();
      unsigned cj;
      {
        const char* pc;
        unsigned len;
        asm volatile("s_getpc_b64 %0\n55:\n\ts_mov_b32 %1, 56f-55b\n\ts_max_u32 %1, %1, 57f-55b" : "=s"(pc), "=s"(len)::"scc");
        asm volatile("global_load_dwordx2 %0, %2, %3 sc1\n\tglobal_load_dword %1, %4, %5" : "=&v"(tag), "=&v"(cj)
                     : "v"(0), "s"(ws + EP_OFF), "v"(min(((i5 * 4 + tt) * 4 + (l & 3)) * 128u, len)), "s"(pc) : "memory");
      }
      const int sx = (r >> 1) & 7, swr = ((q4 >> 1) & 1) | ((c4 & 1) << 1);
      unsigned xa[2][2][4], wa[2];
#pragma unroll
      for (int h2 = 0; h2 < 2; h2++) {
#pragma unroll
        for (int u = 0; u < 8; u++) xa[h2][u >> 2][u & 3] = 32768 + h2 * 65536 + ((u & 3) ^ tt) * 2048 + r * 128 + (((4 * (u >> 2) + c4) ^ sx) << 4);
        wa[h2] = 32768 + h2 * 65536 + 8192 + (8 * c4 + q4) * 128 + ((tt ^ swr) << 5) + p4 * 8;
      }
      unsigned dn = DONE + tt * 4, zr = 0;
      f32x4 acc[3][4] = {}, cac[3] = {}, sac = {}, gac = {};
      bf16x8 ones = __builtin_bit_cast(bf16x8, u32x4{0x3F803F80u, 0x3F803F80u, 0x3F803F80u, 0x3F803F80u});
      bf16x8 xf[2][4], wf[2][3];
      unsigned rdy;
      auto rdyld = [&](unsigned a, unsigned c) {
        const u32x4 d = *(const volatile LAS u32x4*)(size_t)(a + RDY);
        return min(min(d.x, d.y), min(d.z, d.w));
      };
      u32x4 rdy4;
      auto rd = [&](int k, int p) {
        const int u = k & 1, h2 = (k >> 2) & 1, o = ((k >> 1) & 1) * 32768;
        if (p == 0) xf[u][0] = ldsr16(xa[h2][u][0] + o);
        else if (p < 4) wf[u][p - 1] = cat8(trread(wa[h2] + o + (p - 1) * 8192 + u * 4096), trread(wa[h2] + o + (p - 1) * 8192 + u * 4096 + 512));
        else xf[u][p - 3] = ldsr16(xa[h2][u][p - 3] + o);
        SB0();
      };
      auto need = [&](unsigned c) {
        if (UNLIKELY(__builtin_amdgcn_readfirstlane(rdy) <= c)) {
#pragma unroll 1
          for (unsigned s = 0; s < SPIN_MAX; s++) {
            rdy = rdyld(0, c);
            if (__builtin_amdgcn_readfirstlane(rdy) > c) break;
          }
        }
        asm volatile("" ::: "memory");
      };
      auto mm = [&](int k, unsigned c0, bool last) {
        const int u = k & 1;
        const bool pre = !last;
        wait_l<3>();
        MF(sac, ones, xf[u][0]);
        MF(cac[0], wf[u][0], ones);
        if (u && pre) rdy = min(min(rdy4.x, rdy4.y), min(rdy4.z, rdy4.w));
        if (u && pre) need(c0 + (k >> 1) + 1);
        MF(gac, xf[u][0], xf[u][0]);
        MF(acc[0][0], wf[u][0], xf[u][0]); if (pre) rd(k + 1, 0);
        MF(cac[1], wf[u][1], ones); if (pre) rd(k + 1, 1);
        MF(acc[1][0], wf[u][1], xf[u][0]);
        MF(cac[2], ones, wf[u][2]); if (pre) rd(k + 1, 2);
        MF(acc[2][0], xf[u][0], wf[u][2]);
        MF(acc[0][1], wf[u][0], xf[u][1]); if (pre) rd(k + 1, 3);
        MF(acc[1][1], wf[u][1], xf[u][1]);
        if (!u) { rdy4 = *(const volatile LAS u32x4*)(size_t)(zr + RDY); SB0(); }
        MF(acc[2][1], xf[u][1], wf[u][2]); if (pre) rd(k + 1, 4);
        MF(acc[0][2], wf[u][0], xf[u][2]); if (pre) rd(k + 1, 5);
        MF(acc[1][2], wf[u][1], xf[u][2]); if (pre) rd(k + 1, 6);
        MF(acc[2][2], xf[u][2], wf[u][2]);
        MF(acc[0][3], wf[u][0], xf[u][3]);
        if (u && c0 == 0) { *(volatile LAS unsigned*)(size_t)dn = (k >> 1) + 1; SB0(); }
        MF(acc[1][3], wf[u][1], xf[u][3]);
        MF(acc[2][3], xf[u][3], wf[u][2]);
        SB0();
      };
      asm volatile("" : "+v"(acc[0][0]), "+v"(acc[0][1]), "+v"(acc[0][2]), "+v"(acc[0][3]), "+v"(acc[1][0]), "+v"(acc[1][1]), "+v"(acc[1][2]), "+v"(acc[1][3]),
                   "+v"(acc[2][0]), "+v"(acc[2][1]), "+v"(acc[2][2]), "+v"(acc[2][3]), "+v"(cac[0]), "+v"(cac[1]), "+v"(cac[2]), "+v"(ones),
                   "+v"(sac), "+v"(gac), "+v"(wa[0]), "+v"(wa[1]), "+v"(dn), "+v"(zr));
      asm volatile("" : "+v"(xa[0][0][0]), "+v"(xa[0][0][1]), "+v"(xa[0][0][2]), "+v"(xa[0][0][3]), "+v"(xa[0][1][0]), "+v"(xa[0][1][1]), "+v"(xa[0][1][2]), "+v"(xa[0][1][3]),
                   "+v"(xa[1][0][0]), "+v"(xa[1][0][1]), "+v"(xa[1][0][2]), "+v"(xa[1][0][3]), "+v"(xa[1][1][0]), "+v"(xa[1][1][1]), "+v"(xa[1][1][2]), "+v"(xa[1][1][3]));
      rdy = 0u;
      need(0);
#pragma unroll
      for (int p = 0; p < 7; p++) rd(0, p);
#pragma unroll
      for (int k = 0; k < 16; k++) mm(k & 7, (k >> 3) * 4, k == 15);
      {
        asm volatile("" : "+v"(sac), "+v"(gac));
        const float dg = (r & 2) ? ((r & 1) ? gac.w : gac.z) : ((r & 1) ? gac.y : gac.x);
        const float mean = sac.x * (1.0f / D_);
        const float rstd = __builtin_amdgcn_rsqf(fmaxf(dg * (1.0f / D_) - mean * mean, 0.f) + LN_EPS);
        if (c4 == q4) ldsw<f32x2>((16 * tt + r) * 8, f32x2{-mean * rstd, rstd});
      }
      asm volatile("s_waitcnt vmcnt(0) lgkmcnt(0)\n\ts_barrier\n\ts_nop 4" : "+v"(tag), "+v"(cj), "+v"(cac[0]), "+v"(cac[1]), "+v"(cac[2]),
                   "+v"(acc[0][0]), "+v"(acc[0][1]), "+v"(acc[0][2]), "+v"(acc[0][3]), "+v"(acc[1][0]), "+v"(acc[1][1]), "+v"(acc[1][2]), "+v"(acc[1][3]),
                   "+v"(acc[2][0]), "+v"(acc[2][1]), "+v"(acc[2][2]), "+v"(acc[2][3])::"memory");
      const int head = head0 + tt;
      const unsigned so = (l * 16) ^ ((tt & 1) * 256);
      f32x2 st[4];
      f32x4 sv[4][2];
#pragma unroll
      for (int j = 0; j < 4; j++) {
        const unsigned fin = (j ^ tt) * 128;
        st[j] = ldsr<f32x2>(fin + r * 8); sv[j][0] = ldsr<f32x4>(fin + 32 * c4); sv[j][1] = ldsr<f32x4>(fin + 32 * c4 + 16);
      }
#pragma unroll
      for (int jp = 0; jp < 2; jp++) {
        char* kd = ws + KV_OFF + (size_t)(head * 64 + row0 / 32 + (jp ^ (tt >> 1))) * 2048;
        bf16x4 o[2][2];
#pragma unroll
        for (int e = 0; e < 2; e++) {
          const int j = 2 * jp + e;
          o[0][e] = pack4(acc[1][j] * st[j].y + st[j].x * cac[1]);
          const f32x4 mm4 = {sv[j][0].x, sv[j][0].z, sv[j][1].x, sv[j][1].z}, rr4 = {sv[j][0].y, sv[j][0].w, sv[j][1].y, sv[j][1].w};
          o[1][e] = pack4(acc[2][j] * rr4 + mm4 * cac[2].x);
        }
        gst16_sc1(swap_pair(o[0][0], o[0][1]), kd, so);
        gst16_sc1(swap_pair(o[1][0], o[1][1]), kd + 1024, so);
      }
#pragma unroll
      for (int jp = 0; jp < 2; jp++) {
        const f32x2 s0 = st[2 * jp] * QSCALE, s1 = st[2 * jp + 1] * QSCALE;
        gst16_sc1(swap_pair(pack4(acc[0][2 * jp] * s0.y + s0.x * cac[0]), pack4(acc[0][2 * jp + 1] * s1.y + s1.x * cac[0])),
                  ws + QF_OFF + (size_t)(head * 64 + row0 / 32 + (jp ^ (tt >> 1))) * 1024, so);
      }
    }
    tag.x = __builtin_amdgcn_readfirstlane(tag.x) + 1u;
    tag.y = __builtin_amdgcn_readfirstlane(tag.y);
    if (!kh) {
      wait_v<0>();
      if (l == 0) asm volatile("global_store_dwordx2 %0, %1, %2 sc1" ::"v"(0), "v"(tag), "s"(ws + KF_OFF + (head0 + tt) * 4096 + (row0 >> 6) * 128) : "memory");
    }
  }
  const int rt = i5 & 3, nt = i5 >> 2;
  const int rg = i5 & 1, ct = i5 >> 1;
  const unsigned wpo = lr * 1024 + (((((lc >> 1) ^ ((lr >> 1) & 3)) << 1) | (lc & 1)) << 4);
  const int nun = ct < 14 ? 3 : ct == 14 ? 1 : 0;
  const int t16 = xcd * 16 + rt * 4 + tt;
  const char* w1b = (const char*)w1 + ct * 192;
  auto w1off = [&](int u) {
    const int g = u * 64 + l, rr = g / 12, ch = g % 12;
    const int sch = (((ch >> 1) ^ ((rr >> 2) & 1)) << 1) | (ch & 1);
    return (unsigned)(rr * (F_ * 2) + (sch < nun * 4 ? sch : (sch & 3)) * 16);
  };
  auto apos = [&](int u) { return u == 0 ? ct : u * 14 + 1 + ct; };
  bf16x8 rx[2] = {};
  unsigned rxo = r * 1024 + (c4 & 1) * 32 + (c4 >> 1) * 16;
  {
    const int head = (xcd & 1) * 16 + (xcd >> 1) * 4 + (i5 & 3), rb0 = (i5 >> 2) * 8 + tt * 2 + kh;
    const int r32 = l & 31;
    const unsigned cv = ((r32 & 27) == 16) ? 0x3F803F80u : 0u;
    bf16x8 va0 = __builtin_bit_cast(bf16x8, u32x4{cv, cv, cv, cv}), va1 = va0, vb0 = va0, vb1 = va0;
    unsigned la = l * 16 + 32768 + kh * 2048;
    const f32x16 zero = {};
    f32x16 o0 = zero, o1 = zero;
    int idx = 0xDDDD;
    unsigned ga = l * 16 + 49152, gb = wpo, gc = w1off(wv < 6 ? wv >> 1 : 0), po = l * 16;
    unsigned to = min((i5 * 8 + wv) * 43 + (l < 11 ? l : 10), 11008 - 44) * 128;
    u32x4 pz = {POISON, POISON, POISON, POISON};
    asm volatile("" : "+v"(o0), "+v"(o1), "+v"(va0), "+v"(va1), "+v"(vb0), "+v"(vb1), "+v"(idx), "+v"(la), "+v"(ga), "+v"(gb), "+v"(gc), "+v"(po), "+v"(to), "+v"(pz), "+v"(rxo), "+v"(rx[0]), "+v"(rx[1]));
    u32x2 fj[3] = {};
    asm volatile("" : "+v"(fj[0]), "+v"(fj[1]), "+v"(fj[2]));
    if (wv == 4) {
      __builtin_amdgcn_s_sleep(K_SLEEP);
      flag_spin3<P3_GAP>(ws + KF_OFF + head * 4096, r32 * 128, ((unsigned long long)tag.y << 32) | tag.x, fj);
    }
    asm volatile("s_barrier" ::: "memory");
    bf16x8 q0, q1;
    const char* src = ws + KV_OFF + (size_t)head * 131072 + wv * 1024;
    glds16_sc1(src, l * 16, 32768 + wv * 1024);
    gld16_sc1(q0, ws + QF_OFF + (size_t)(head * 64 + rb0) * 1024, l * 16);
    gld16_sc1(q1, ws + QF_OFF + (size_t)(head * 64 + (rb0 ^ 1)) * 1024, l * 16);
    __builtin_amdgcn_s_sleep(4);
#pragma unroll
    for (int p = 1; p < 6; p++) glds16_sc1(src + p * 8192, l * 16, 32768 + p * 8192 + wv * 1024);
    asm volatile("s_waitcnt vmcnt(5)\n\ts_barrier" : "+v"(q0), "+v"(q1)::"memory");
    asm volatile("" : "+v"(fj[0]), "+v"(fj[1]), "+v"(fj[2]));
    f32x16 c0, c1, sb;
    bf16x8 kf = ldsr16(la);
    {
      const bf16x8 k0 = ldsr16(32768 + l * 16);
      if (r32 < 16) { va0 = ldsr16(la + 1024); va1 = ldsr16(la + 1280); }
      const float m0 = max16(mfma32(k0, q0, zero)), m1 = max16(mfma32(k0, q1, zero));
#pragma unroll
      for (int i = 0; i < 16; i++) { c0[i] = -m0; c1[i] = -m1; }
    }
    f32x16 sa = mfma32(kf, q0, c0);
    sa[0] = __builtin_amdgcn_exp2f(sa[0]);
    sa[1] = __builtin_amdgcn_exp2f(sa[1]);
    asm volatile("s_waitcnt vmcnt(0)\n\ts_barrier" : "+{v[96:111]}"(sa), "+v"(c0), "+v"(c1)::"memory");
    const unsigned long long vmask = 0x0000FFFF0000FFFFull;
    {
      int n = 5;
      unsigned keep;
      asm volatile("s_mov_b32 %[kp], m0\n\ts_mov_b32 m0, %[d0]\n\t" ATT_BODY(ATT_DMS, ATT_DMS, "", "") "s_mov_b32 m0, %[kp]\n\ts_waitcnt vmcnt(0)\n\ts_barrier\n\ts_nop 11"
                   : ATT_OUT, [kp] "=&s"(keep), [g] "+v"(ga)
                   : ATT_IN(PS_A), [src] "s"(src), [gs] "i"(8192), [d0] "s"(32768 + 49152 + wv * 1024), [ds] "i"(8192) : "memory", "scc");
    }
    unsigned junk;
    {
      int n = 8;
      unsigned keep;
      const char* tb;
      asm volatile("s_mov_b32 %[kp], m0\n\ts_mov_b32 m0, %[d0]\n\t"
                   ATT_BODY(ATT_PZ, ATT_DMA, "s_cmp_gt_u32 %[n], 4\n\ts_cselect_b64 %[tb], %[w1], %[w2]\n\tglobal_load_dword %[jk], %[to], %[tb]\n\tv_add_u32 %[to], 0x580, %[to]\n\t", "")
                   "s_mov_b32 m0, %[kp]\n\ts_waitcnt vmcnt(2)\n\ts_barrier\n\ts_nop 11"
                   : ATT_OUT, [kp] "=&s"(keep), [g] "+v"(gb), [jk] "=&v"(junk), [po] "+v"(po), [to] "+v"(to), [tb] "=&s"(tb)
                   : ATT_IN(PS_B), [src] "s"((const char*)wo + wv * 8192 + nt * 128), [gs] "i"(65536), [d0] "s"(wv * 1024), [ds] "i"(8192),
                     [w1] "s"(w1), [w2] "s"((const char*)w2 - 4 * 0x580), [pz] "v"(pz), [pb] "s"(ws + OF_OFF + (size_t)(b * 8 + wv) * 5120), [pm] "s"(0xFFFFFFFFFFull)
                   : "memory", "scc");
    }
    if (tid == 0) __hip_atomic_store((GAS unsigned*)(ws + PF_OFF) + b, tag.x, __ATOMIC_RELAXED, __HIP_MEMORY_SCOPE_AGENT);
    u32x4 pf;
    if (kh) {
      gld16(rx[0], (const char*)x + (size_t)t16 * 16384 + nt * 128, rxo);
      gld16(rx[1], (const char*)x + (size_t)t16 * 16384 + nt * 128 + 64, rxo);
    }
    {
      int n = 3;
      unsigned keep;
      asm volatile("s_mov_b32 %[kp], m0\n\ts_mov_b32 m0, %[d0]\n\t"
                   ATT_BODY(ATT_DMA ATT_DMA, ATT_DMA, "s_cmp_eq_u32 %[n], %[fi]\n\ts_cbranch_scc0 9f\n\tglobal_load_dwordx4 %[pf], %[fo], %[fb] sc1\n9:\n\t", "")
                   "s_mov_b32 m0, %[kp]\n\ts_waitcnt vmcnt(0)\n\ts_nop 11"
                   : ATT_OUT, [kp] "=&s"(keep), [g] "+v"(gc), [pf] "=&v"(pf), "+v"(junk), "+v"(rx[0]), "+v"(rx[1])
                   : ATT_IN(PS_C), [src] "s"((nun ? w1b : (const char*)w1) + (wv & 1) * 16 * F_ * 2), [gs] "i"(2 * 16 * F_ * 2),
                     [d0] "s"(nun && wv < 6 ? 65536 + (wv & 1) * 3072 + (wv >> 1) * 1024 : 163840), [ds] "i"(6144), [fi] "s"((wv + 1) >> 3), [fo] "v"(l * 16), [fb] "s"(ws + PF_OFF)
                   : "memory", "scc");
    }
    {
      const unsigned mb = 120832 + wv * 2304, mo = 120832 + (wv ^ 4) * 2304;
      ldsw<f32x4>(mb + l * 16, f32x4{o1[0], o1[1], o1[2], o1[3]});
      ldsw<f32x4>(mb + 1024 + l * 16, f32x4{o1[4], o1[5], o1[6], o1[7]});
      ldsw<float>(mb + 2048 + l * 4, o1[8]);
      if (wv == 7) {
#pragma unroll 1
        for (unsigned s = 0; s < SPIN_MAX; s++) {
          if (__builtin_amdgcn_ballot_w64(pf.x != tag.x || pf.y != tag.x || pf.z != tag.x || pf.w != tag.x) == 0) break;
          asm volatile("global_load_dwordx4 %0, %1, %2 sc1\n\ts_waitcnt vmcnt(0)" : "=&v"(pf) : "v"(l * 16), "s"(ws + PF_OFF) : "memory");
        }
        if (b == 0 && l == 0) asm volatile("global_store_dwordx2 %0, %1, %2 sc1" ::"v"(0), "v"(tag), "s"(ws + EP_OFF) : "memory");
      }
      bar_l();
      const f32x4 a = ldsr<f32x4>(mo + l * 16), c = ldsr<f32x4>(mo + 1024 + l * 16);
#pragma unroll
      for (int i = 0; i < 4; i++) { o0[i] += a[i]; o0[4 + i] += c[i]; }
      o0[8] += ldsr<float>(mo + 2048 + l * 4);
    }
    float lsum = o0[8];
    float chk = 0.f;
#pragma unroll
    for (int i = 0; i < 8; i++) chk = fmaf(o0[i], 0.f, chk);
    if (UNLIKELY(!__all(lsum < 1e18f && chk == 0.f))) {
      float m = -INFINITY;
      o0 = zero;
      const char* kv = ws + KV_OFF + (size_t)head * 131072;
#pragma unroll 1
      for (int t = 0; t < 64; t++) {
        bf16x8 kk, v0, v1;
        asm volatile("global_load_dwordx4 %0, %3, %4 sc1\n\tglobal_load_dwordx4 %1, %3, %4 offset:1024 sc1\n\tglobal_load_dwordx4 %2, %3, %4 offset:1280 sc1\n\ts_waitcnt vmcnt(0)"
                     : "=&v"(kk), "=&v"(v0), "=&v"(v1) : "v"(l * 16), "s"(kv + t * 2048) : "memory");
        const f32x16 sv = mfma32(kk, q0, zero);
        if (r32 < 16) { va0 = v0; va1 = v1; }
        const float mn = fmaxf(m, max16(sv));
        const float al = __builtin_amdgcn_exp2f(m - mn);
        m = mn;
        f32x16 p;
#pragma unroll
        for (int i = 0; i < 16; i++) p[i] = __builtin_amdgcn_exp2f(sv[i] - mn);
        o0 = pv(va0, va1, p, o0 * al);
      }
      lsum = o0[8];
    }
    const float inv = __builtin_amdgcn_rcpf(lsum);
    bf16x8 ov;
#pragma unroll
    for (int i = 0; i < 8; i++) ov[i] = (__bf16)(o0[i] * inv);
    gst16_sc1(ov, ws + OF_OFF + (size_t)(rb0 * 32 + head) * 1024, l * 16);
    asm volatile("s_barrier" ::: "memory");
  }
  if (kh == 0) {
    if (nun == 0) return;
    if (tt < 3) glds_seq<14, 16 * F_ * 2, 3072>(w1b + 18 * 16 * F_ * 2, w1off(tt), 65536 + 18 * 3072 + tt * 1024);
    const unsigned rowl = 65536 + (4 * c4 + q4) * 192 + p4 * 8;
    unsigned wb[2][2] = {{rowl + (c4 & 1) * 32, rowl + 32 - (c4 & 1) * 32}, {rowl + 49152 + (c4 & 1) * 32, rowl + 49152 + 32 - (c4 & 1) * 32}};
    bf16x4 pre[6];
#pragma unroll
    for (int j = 0; j < 6; j++) gld8(pre[j], (const char*)b1 + ct * 192 + (j < 2 * nun ? j : (j & 1)) * 32, c4 * 8);
    asm volatile("s_waitcnt vmcnt(6)\n\ts_barrier" : "+v"(wb[0][0]), "+v"(wb[0][1]), "+v"(wb[1][0]), "+v"(wb[1][1])::"memory");
    if (tt < 3) {
      const bf16x8 ones = __builtin_bit_cast(bf16x8, u32x4{0x3F803F80u, 0x3F803F80u, 0x3F803F80u, 0x3F803F80u});
      f32x4 s0 = {}, s1 = {};
      bf16x8 f[16][2];
#pragma unroll
      for (int k = 0; k < 16; k++) {
        const unsigned a = wb[k >> 3][0] + tt * 64 + (k & 7) * 6144, c = wb[k >> 3][1] + tt * 64 + (k & 7) * 6144;
        f[k][0] = cat8(trread(a), trread(a + 3072));
        f[k][1] = cat8(trread(c), trread(c + 3072));
      }
      SB0();
#pragma unroll
      for (int k = 0; k < 16; k++) { s0 = mfma16(f[k][0], ones, s0); s1 = mfma16(f[k][1], ones, s1); }
      if (r == 0) { ldsw<f32x4>(tt * 128 + c4 * 16, s0); ldsw<f32x4>(tt * 128 + 64 + c4 * 16, s1); }
    }
    bf16x4 wf[2][6][2];
    bf16x8 af[16][2];
    auto rd = [&](int k, int j, int t) { wf[k & 1][j][t] = trread(wb[k >> 3][j & 1] + (j & 6) * 32 + (k & 7) * 6144 + t * 3072); SB0(); };
#pragma unroll
    for (int j = 0; j < 12; j++) rd(0, j >> 1, j & 1);
    asm volatile("s_waitcnt vmcnt(0)" : "+v"(pre[0]), "+v"(pre[1]), "+v"(pre[2]), "+v"(pre[3]), "+v"(pre[4]), "+v"(pre[5])::"memory");
    f32x4 bl[6];
#pragma unroll
    for (int j = 0; j < 6; j++) bl[j] = up4(pre[j]) * LOG2E;
    f32x4 acc[2][6] = {};
    asm volatile("" : "+v"(acc[0][0]), "+v"(acc[0][1]), "+v"(acc[0][2]), "+v"(acc[0][3]), "+v"(acc[0][4]), "+v"(acc[0][5]),
                 "+v"(acc[1][0]), "+v"(acc[1][1]), "+v"(acc[1][2]), "+v"(acc[1][3]), "+v"(acc[1][4]), "+v"(acc[1][5]),
                 "+v"(bl[0]), "+v"(bl[1]), "+v"(bl[2]), "+v"(bl[3]), "+v"(bl[4]), "+v"(bl[5]));
    bar_l();
    f32x4 cs[6];
#pragma unroll
    for (int j = 0; j < 6; j++) cs[j] = ldsr<f32x4>(j * 64 + 16 * c4);
    const int u16 = xcd * 16 + rg * 8 + tt * 2;
    const char* ab = ws + HF_OFF + (size_t)u16 * 16384;
    const char* sbp = ws + ST_OFF + (size_t)u16 * 1024;
    __builtin_amdgcn_s_sleep(H_SLEEP);
    f32x2 sp[2][2];
#pragma unroll 1
    for (unsigned s = 0; s < SPIN_MAX; s++) {
      asm volatile("global_load_dwordx2 %0, %4, %5 sc1\n\tglobal_load_dwordx2 %1, %4, %5 offset:128 sc1\n\tglobal_load_dwordx2 %2, %4, %5 offset:1024 sc1\n\t"
                   "global_load_dwordx2 %3, %4, %5 offset:1152 sc1\n\ts_waitcnt vmcnt(0)"
                   : "=&v"(sp[0][0]), "=&v"(sp[0][1]), "=&v"(sp[1][0]), "=&v"(sp[1][1]) : "v"(c4 * 256 + r * 8), "s"(sbp) : "memory");
      const u32x4 a = __builtin_bit_cast(u32x4, __builtin_shufflevector(sp[0][0], sp[0][1], 0, 1, 2, 3)), c = __builtin_bit_cast(u32x4, __builtin_shufflevector(sp[1][0], sp[1][1], 0, 1, 2, 3));
      if (!any_poison(max(max(max(a.x, a.y), max(a.z, a.w)), max(max(c.x, c.y), max(c.z, c.w))))) break;
      __builtin_amdgcn_s_sleep(1);
    }
    constexpr int WIN = 7;
    static_for<0, WIN>([&](auto ic) __attribute__((always_inline)) { constexpr int k = decltype(ic)::v; frag_sc1<k>(af[k][0], ab, l * 16); frag_sc1<16 + k>(af[k][1], ab, l * 16); });
    float rl[2], ml[2];
#pragma unroll
    for (int t = 0; t < 2; t++) {
      const f32x2 s2 = sp[t][0] + sp[t][1];
      const float mean = red_c(s2.x) * (1.0f / D_);
      rl[t] = __builtin_amdgcn_rsqf(fmaxf(red_c(s2.y) * (1.0f / D_) - mean * mean, 0.f) + LN_EPS) * LOG2E;
      ml[t] = -mean * rl[t];
    }
    f32x4 cb[2][6];
#pragma unroll
    for (int j = 0; j < 6; j++) { cb[0][j] = ml[0] * cs[j] + bl[j]; cb[1][j] = ml[1] * cs[j] + bl[j]; }
    asm volatile("" : "+v"(cb[0][0]), "+v"(cb[0][1]), "+v"(cb[0][2]), "+v"(cb[0][3]), "+v"(cb[0][4]), "+v"(cb[0][5]),
                 "+v"(cb[1][0]), "+v"(cb[1][1]), "+v"(cb[1][2]), "+v"(cb[1][3]), "+v"(cb[1][4]), "+v"(cb[1][5]), "+v"(rl[0]), "+v"(rl[1]));
    static_for<0, 16>([&](auto ic) __attribute__((always_inline)) {
      constexpr int k = decltype(ic)::v;
      asm volatile("s_waitcnt vmcnt(%2)" : "+v"(af[k][0]), "+v"(af[k][1]) : "i"(k + WIN <= 16 ? 2 * WIN - 2 : 2 * (15 - k)) : "memory");
#pragma unroll
      for (int j = 0; j < 6; j++) {
        if (j == 0) wait_l<6>();
        if (j == 3) { if (k < 15) wait_l<6>(); else wait_l<0>(); }
        const bf16x8 wv8 = cat8(wf[k & 1][j][0], wf[k & 1][j][1]);
        MF(acc[0][j], wv8, af[k][0]);
        if (k < 15) rd(k + 1, j, 0);
        if constexpr (k + WIN < 16) { if (j == 1) frag_sc1<k + WIN>(af[k + WIN][0], ab, l * 16); if (j == 4) frag_sc1<16 + k + WIN>(af[k + WIN][1], ab, l * 16); }
        MF(acc[1][j], wv8, af[k][1]);
        if (k < 15) rd(k + 1, j, 1);
      }
    });
    asm volatile("s_nop 7" : "+v"(acc[0][0]), "+v"(acc[0][1]), "+v"(acc[0][2]), "+v"(acc[0][3]), "+v"(acc[0][4]), "+v"(acc[0][5]),
                 "+v"(acc[1][0]), "+v"(acc[1][1]), "+v"(acc[1][2]), "+v"(acc[1][3]), "+v"(acc[1][4]), "+v"(acc[1][5]));
    if (UNLIKELY(any_nan(acc[0][0].x + acc[1][0].x))) {
#pragma unroll
      for (int j = 0; j < 6; j++) acc[0][j] = acc[1][j] = f32x4{};
#pragma unroll 1
      for (int k = 0; k < 16; k++) {
        bf16x8 a0, a1;
        refetch(a0, ab + k * 1024, l * 16);
        refetch(a1, ab + 16384 + k * 1024, l * 16);
#pragma unroll
        for (int j = 0; j < 6; j++) {
          const unsigned a = wb[0][j & 1] + (j & 6) * 32 + k * 6144;
          const bf16x8 wv8 = cat8(trread(a), trread(a + 3072));
          acc[0][j] = mfma16(wv8, a0, acc[0][j]);
          acc[1][j] = mfma16(wv8, a1, acc[1][j]);
        }
      }
    }
    bar_l();
#pragma unroll
    for (int u = 0; u < 3; u++) {
      if (u < nun) {
        f32x4 v[4];
#pragma unroll
        for (int k = 0; k < 4; k++) v[k] = acc[k >> 1][2 * u + (k & 1)] * rl[k >> 1] + cb[k >> 1][2 * u + (k & 1)];
        SB0();
        mish_n<4>(v);
        SB0();
        gst16_sc1(cat8(pack4(v[0]), pack4(v[1])), ws + AF_OFF + (size_t)(u16 * 43 + apos(u)) * 1024, l * 16);
        gst16_sc1(cat8(pack4(v[2]), pack4(v[3])), ws + AF_OFF + (size_t)((u16 + 1) * 43 + apos(u)) * 1024, l * 16);
      }
    }
    asm volatile("56:");
    return;
  }
  const int swp = ((c4 & 1) << 1) | (q4 >> 1);
  f32x4 acc[4];
  bf16x4 hb[4];
  {
    bf16x4 pre[4];
#pragma unroll
    for (int e = 0; e < 2; e++) {
      const bf16x8 t = swap_pair(__builtin_shufflevector(rx[e], rx[e], 0, 1, 2, 3), __builtin_shufflevector(rx[e], rx[e], 4, 5, 6, 7));
      pre[2 * e] = __builtin_shufflevector(t, t, 0, 1, 2, 3);
      pre[2 * e + 1] = __builtin_shufflevector(t, t, 4, 5, 6, 7);
    }
    unsigned wa[4];
#pragma unroll
    for (int j = 0; j < 4; j++) wa[j] = (16 * (c4 >> 1) + 4 * (c4 & 1) + q4) * 128 + ((j ^ swp) << 5) + p4 * 8;
    constexpr int WR = 6;
    bf16x8 wf[WR][4], af[16];
    auto rd = [&](int k, int j) { const unsigned a = wa[j] + k * 4096; wf[k % WR][j] = cat8(trread(a), trread(a + 1024)); SB0(); };
#pragma unroll
    for (int k = 0; k < WR; k++)
#pragma unroll
      for (int j = 0; j < 4; j++) rd(k, j);
    const char* ab = ws + OF_OFF + (size_t)(t16 >> 1) * 32768 + (t16 & 1) * 256;
    const unsigned ao = (c4 >> 1) * 1024 + (c4 & 1) * 512 + r * 16;
#pragma unroll
    for (int j = 0; j < 4; j++) acc[j] = up4(pre[j]);
    asm volatile("s_waitcnt lgkmcnt(0)\n\ts_barrier" : "+v"(acc[0]), "+v"(acc[1]), "+v"(acc[2]), "+v"(acc[3])::"memory");
    __builtin_amdgcn_s_setprio(1);
    hint_spin(ab, (l & 31) * 1024);
    static_for<0, 16>([&](auto ic) __attribute__((always_inline)) { constexpr int k = decltype(ic)::v; frag_sc1<2 * k>(af[k], ab, ao); });
    wait_l<0>();
    static_for<0, 16>([&](auto ic) __attribute__((always_inline)) {
      constexpr int k = decltype(ic)::v;
      asm volatile("s_waitcnt vmcnt(%1)" : "+v"(af[k]) : "i"(15 - k) : "memory");
      if (k == WR || k == WR + 4) wait_l<8>();
      if (k == 13) wait_l<0>();
#pragma unroll
      for (int j = 0; j < 4; j++) {
        MF(acc[j], wf[k % WR][j], af[k]);
        if (k + WR < 16) rd(k + WR, j);
      }
    });
    asm volatile("s_nop 7" : "+v"(acc[0]), "+v"(acc[1]), "+v"(acc[2]), "+v"(acc[3]));
    if (UNLIKELY(any_nan(acc[0].x))) {
#pragma unroll
      for (int j = 0; j < 4; j++) acc[j] = up4(pre[j]);
#pragma unroll 1
      for (int k = 0; k < 16; k++) {
        bf16x8 a0;
        refetch(a0, ab + k * 2048, ao);
#pragma unroll
        for (int j = 0; j < 4; j++) {
          bf16x8 wv8;
#pragma unroll
          for (int e = 0; e < 8; e++) wv8[e] = wo[(size_t)(32 * k + 16 * (c4 >> 1) + 4 * (c4 & 1) + (e & 3) + 8 * (e >> 2)) * D_ + nt * 64 + j * 16 + r];
          acc[j] = mfma16(wv8, a0, acc[j]);
        }
      }
    }
#pragma unroll
    for (int j = 0; j < 4; j++) hb[j] = pack4(acc[j]);
    gst16_sc1(cat8(hb[0], hb[1]), ws + HF_OFF + (size_t)(t16 * 16 + nt * 2) * 1024, l * 16);
    gst16_sc1(cat8(hb[2], hb[3]), ws + HF_OFF + (size_t)(t16 * 16 + nt * 2 + 1) * 1024, l * 16);
#pragma unroll
    for (int j = 0; j < 4; j++) acc[j] = up4(hb[j]);
    const f32x4 hs = (acc[0] + acc[1]) + (acc[2] + acc[3]), hq = (acc[0] * acc[0] + acc[1] * acc[1]) + (acc[2] * acc[2] + acc[3] * acc[3]);
    const f32x2 st = {red_c((hs.x + hs.y) + (hs.z + hs.w)), red_c((hq.x + hq.y) + (hq.z + hq.w))};
    if (c4 == 0) asm volatile("global_store_dwordx2 %0, %1, %2 sc1" ::"v"(r * 8), "v"(st), "s"(ws + ST_OFF + (size_t)(t16 * 8 + nt) * 128) : "memory");
    asm volatile("s_barrier" : "+v"(acc[0]), "+v"(acc[1]), "+v"(acc[2]), "+v"(acc[3])::"memory");
    __builtin_amdgcn_s_setprio(0);
  }
  {
    const char* w2b = (const char*)w2 + nt * 128 + tt * 8192;
    glds_seq<15, 3 * 32768, 4096>(w2b, wpo, 4096 + tt * 1024);
    bf16x8 pk[28];
    static_for<0, 28>([&](auto ic) __attribute__((always_inline)) {
      constexpr int e = decltype(ic)::v;
      if (e == PARK_SPLIT) __builtin_amdgcn_s_sleep(PARK_SLEEP);
      gld16(pk[e], w2b + (e < 14 ? 3 * e + 1 : 3 * (e - 14) + 2) * 32768, wpo);
    });
    unsigned wa[3][4];
#pragma unroll
    for (int g = 0; g < 3; g++)
#pragma unroll
      for (int j = 0; j < 4; j++) wa[g][j] = g * 65536 + (4 * c4 + q4) * 128 + ((j ^ swp) << 5) + p4 * 8;
    unsigned pw[2] = {(unsigned)(65536 + tt * 1024 + l * 16), (unsigned)(131072 + tt * 1024 + l * 16)};
    asm volatile("s_waitcnt vmcnt(28)\n\ts_barrier" : "+v"(wa[0][0]), "+v"(wa[0][1]), "+v"(wa[0][2]), "+v"(wa[0][3]), "+v"(wa[1][0]), "+v"(wa[1][1]), "+v"(wa[1][2]), "+v"(wa[1][3]),
                 "+v"(wa[2][0]), "+v"(wa[2][1]), "+v"(wa[2][2]), "+v"(wa[2][3]), "+v"(pw[0]), "+v"(pw[1])::"memory");
    static_for<0, 7>([&](auto ic) __attribute__((always_inline)) {
      constexpr int e = decltype(ic)::v * 4;
      wait4<24 - e>(pk[e], pk[e + 1], pk[e + 2], pk[e + 3]);
      if (e < 24) {
#pragma unroll
        for (int f = e; f < e + 4; f++) ldsw<bf16x8>(pw[f >> 4] + (f & 15) * 4096, pk[f]);
      }
    });
    bf16x8 wf[3][4], af[43];
    auto rd = [&](int i, int j) { const int sl = i < 39 ? i + 1 : i - 39; const unsigned a = wa[sl >> 4][j] + (sl & 15) * 4096; wf[i % 3][j] = cat8(trread(a), trread(a + 2048)); SB0(); };
#pragma unroll
    for (int j = 0; j < 8; j++) rd(j >> 2, j & 3);
    const char* ab = ws + AF_OFF + (size_t)t16 * 43 * 1024;
    const unsigned ho = (r < 14 ? r : 13) * 1024;
    __builtin_amdgcn_s_sleep(A_SLEEP);
    hint_spin(ab, (r < 15 ? r : 14) * 1024);
    constexpr int WIN = 12;
    unsigned hint;
    static_for<0, WIN>([&](auto ic) __attribute__((always_inline)) {
      constexpr int i = decltype(ic)::v;
      frag_sc1<i>(af[i], ab, l * 16);
      if (i == 7) gld4_sc1(hint, ab + 15 * 1024, ho);
    });
    constexpr auto younger = [](int i) {
      const int last = i - 1 + WIN < 42 ? i - 1 + WIN : 42;
      return last - i + (i <= 7 ? 1 : 0) + (i > 11 && i <= 11 + WIN ? 1 : 0);
    };
    static_for<0, 43>([&](auto ic) __attribute__((always_inline)) {
      constexpr int i = decltype(ic)::v;
      if (i + WIN == 15 || i + WIN == 29) {
        asm volatile("s_waitcnt vmcnt(%1)" : "+v"(hint) : "i"(i + WIN == 15 ? 7 : 5) : "memory");
        if (UNLIKELY(any_poison(hint))) hint_spin(ab + (i + WIN) * 1024, ho);
      }
      if (i == 13) asm volatile("s_barrier" ::: "memory");
      if (i == 34) asm volatile("s_barrier" ::: "memory");
      asm volatile("s_waitcnt vmcnt(%1)" : "+v"(af[i]) : "i"(younger(i)) : "memory");
      if (i >= 13 && i <= 16) wait_l<9>(); else if (i < 42) wait_l<8>(); else wait_l<0>();
      MF(acc[0], wf[i % 3][0], af[i]);
      if (i < 41) rd(i + 2, 0);
      MF(acc[1], wf[i % 3][1], af[i]);
      if (i < 41) rd(i + 2, 1);
      if constexpr (i + WIN < 43) frag_sc1<i + WIN>(af[i + WIN], ab, l * 16);
      if (i == 11) gld4_sc1(hint, ab + 29 * 1024, ho);
      MF(acc[2], wf[i % 3][2], af[i]);
      if (i < 41) rd(i + 2, 2);
      MF(acc[3], wf[i % 3][3], af[i]);
      if (i < 41) rd(i + 2, 3);
      if constexpr (i >= 12 && i < 16) { ldsw<bf16x8>((i - 12) * 4096 + tt * 1024 + l * 16, pk[i + 12]); SB0(); }
    });
    asm volatile("s_nop 7" : "+v"(acc[0]), "+v"(acc[1]), "+v"(acc[2]), "+v"(acc[3]));
    if (UNLIKELY(any_nan(acc[0].x))) {
#pragma unroll
      for (int j = 0; j < 4; j++) acc[j] = up4(hb[j]);
#pragma unroll 1
      for (int i = 0; i < 43; i++) {
        bf16x8 a0;
        refetch(a0, ab + i * 1024, l * 16);
        const int ks = i < 15 ? 3 * i : i < 29 ? 3 * i - 44 : 3 * i - 85;
#pragma unroll
        for (int j = 0; j < 4; j++) {
          bf16x8 wv8;
#pragma unroll
          for (int e = 0; e < 8; e++) wv8[e] = w2[(size_t)(32 * ks + 4 * c4 + (e & 3) + 16 * (e >> 2)) * D_ + nt * 64 + j * 16 + r];
          acc[j] = mfma16(wv8, a0, acc[j]);
        }
      }
    }
    char* op = (char*)out + (size_t)t16 * 16384 + nt * 128 + rxo;
    *(bf16x8*)op = swap_pair(pack4(acc[0]), pack4(acc[1]));
    *(bf16x8*)(op + 64) = swap_pair(pack4(acc[2]), pack4(acc[3]));
    asm volatile("57:");
  }
}

extern "C" void kernel_launch(void* const* d_in, const int* in_sizes, int n_in, void* d_out, int out_size, void* d_ws, size_t ws_size, hipStream_t stream) {
  const __bf16* x = (const __bf16*)d_in[0];
  const __bf16* wqkv = (const __bf16*)d_in[1];
  const __bf16* wo = (const __bf16*)d_in[2];
  const __bf16* w1 = (const __bf16*)d_in[4];
  const __bf16* b1 = (const __bf16*)d_in[5];
  const __bf16* w2 = (const __bf16*)d_in[6];
  char* ws = (char*)d_ws;
  (void)hipFuncSetAttribute((const void*)k_block, hipFuncAttributeMaxDynamicSharedMemorySize, LDS_BYTES);
  k_block<<<256, 512, LDS_BYTES, stream>>>(x, wqkv, wo, w1, b1, w2, ws, (__bf16*)d_out);
}
```
